```python
import torch

def _softmax(x, axis=-1):
    return torch.softmax(x, dim=axis)
def _gelu(x):
    return torch.nn.functional.gelu(x, approximate="tanh")
def _silu(x):
    return torch.nn.functional.silu(x)
def _sigmoid(x):
    return torch.sigmoid(x)
def _layernorm(x, axis=-1, eps=1e-5):
    m = x.mean(dim=axis, keepdim=True)
    v = x.var(dim=axis, keepdim=True, unbiased=False)
    return (x - m) / torch.sqrt(v + eps)
def _scatter_add(tgt, idx, src, axis=0):
    return tgt.clone().index_add_(axis, idx, src)
def _segment_sum(src, seg, n):
    out = torch.zeros((n,) + src.shape[1:], dtype=src.dtype)
    return out.index_add_(0, seg, src)
def _pad(x, pw):
    flat = tuple(v for pair in reversed(pw) for v in pair)
    return torch.nn.functional.pad(x, flat)

kernel_name = 'parallel_sum__sliding_block_cached__glu_gated_glu__decode'

def setup_inputs():

    _w = (lambda a: (lambda t: t.to(torch.bfloat16) if t.is_floating_point() else t)(torch.as_tensor(a)))
    def _A_setup():
        B, Sq, D = 1, 1, 448
        Hq, Hkv, Sk, Dh = 16, 8, 65536, 256
        assert Hq % Hkv == 0, (Hq, Hkv)
        return {
            "x": _w((lambda *s: torch.randn(*s, dtype=torch.bfloat16) * 0.05)(B, Sq, D)),
            "k_cache": _w((lambda *s: torch.randn(*s, dtype=torch.bfloat16) * 3.2)(B, Hkv, Sk, Dh)),
            "v_cache": _w((lambda *s: torch.randn(*s, dtype=torch.bfloat16) * 1.6)(B, Hkv, Sk, Dh)),
            "wq": _w((lambda *s: torch.randn(*s, dtype=torch.bfloat16) * 3.2)(D, Hq * Dh) * (D ** -0.5)),
            "wo": _w((lambda *s: torch.randn(*s, dtype=torch.bfloat16) * 1.6)(Hq * Dh, D) * ((Hq * Dh) ** -0.5)),
        }
    _A_in = _A_setup()

    def _B_setup():
        B, S, D, F = 1, 1, 448, 1792
        return {
            "x": _w((lambda *s: torch.randn(*s, dtype=torch.bfloat16) * 0.05)(B, S, D)),
            "w_gate": _w((lambda *s: torch.randn(*s, dtype=torch.bfloat16) * 0.1)(D, F)),
            "w_up": _w((lambda *s: torch.randn(*s, dtype=torch.bfloat16) * 0.05)(D, F)),
            "w_down": _w((lambda *s: torch.randn(*s, dtype=torch.bfloat16) * 0.05)(F, D)),
        }
    _B_in = _B_setup()

    return {
        "A_x": _A_in["x"],
        "A_k_cache": _A_in["k_cache"],
        "A_v_cache": _A_in["v_cache"],
        "A_wq": _A_in["wq"],
        "A_wo": _A_in["wo"],
        "B_x": _B_in["x"],
        "B_w_gate": _B_in["w_gate"],
        "B_w_up": _B_in["w_up"],
        "B_w_down": _B_in["w_down"],
    }

def reference(A_x, A_k_cache, A_v_cache, A_wq, A_wo, B_x, B_w_gate, B_w_up, B_w_down):
    def _A_ref(x, k_cache, v_cache, wq, wo):
        B, Sq, D = x.shape
        Hq, Dh = 16, 256
        Hkv, Sk = k_cache.shape[1], k_cache.shape[2]
        W = min(4096, max(256, Sk // 4))
        rep = Hq // Hkv
        q = (lambda x, *d: x.permute(*d))((x @ wq).reshape(B, Sq, Hq, Dh), 0, 2, 1, 3)
        k = (lambda x, n, axis: torch.repeat_interleave(x, n, dim=axis))(k_cache[:, :, Sk - W:, :], rep, axis=1)
        v = (lambda x, n, axis: torch.repeat_interleave(x, n, dim=axis))(v_cache[:, :, Sk - W:, :], rep, axis=1)
        scores = torch.einsum("bhqd,bhkd->bhqk", q, k) * (Dh ** -0.5)
        p = _softmax(scores, axis=-1)
        o = torch.einsum("bhqk,bhkd->bhqd", p, v)
        return (lambda x, *d: x.permute(*d))(o, 0, 2, 1, 3).reshape(B, Sq, Hq * Dh) @ wo

    def _B_ref(x, w_gate, w_up, w_down):
        g = torch.einsum("bsd,df->bsf", x, w_gate)
        u = torch.einsum("bsd,df->bsf", x, w_up)
        h = _sigmoid(g) * u
        return torch.einsum("bsf,fd->bsd", h, w_down)

    x = A_x
    xn = _layernorm(x, axis=-1)
    return x + _A_ref(xn, A_k_cache, A_v_cache, A_wq, A_wo) + _B_ref(xn, B_w_gate, B_w_up, B_w_down)
```

```cpp
#include <hip/hip_runtime.h>
#include <hip/hip_bf16.h>
#include <cstdint>

constexpr int D = 448, HQ = 16, HKV = 8, SK = 65536, DH = 256, WIN = 4096, F = 1792;
constexpr int NQ = HQ * DH;
constexpr int CH = 128;
constexpr int NCH = WIN / CH;

using u32x4 = __attribute__((ext_vector_type(4))) unsigned;
using bf16x2 = __attribute__((ext_vector_type(2))) __bf16;

__device__ __forceinline__ float bf2f(unsigned short v) { return __uint_as_float(((unsigned)v) << 16); }
__device__ __forceinline__ float rbf(float f) {
  unsigned u = __float_as_uint(f);
  u += 0x7FFFu + ((u >> 16) & 1u);
  return __uint_as_float(u & 0xFFFF0000u);
}
__device__ __forceinline__ float lo16(unsigned v) { return __uint_as_float(v << 16); }
__device__ __forceinline__ float hi16(unsigned v) { return __uint_as_float(v & 0xFFFF0000u); }
__device__ __forceinline__ float dot2(unsigned k, unsigned q, float acc) {
  return __builtin_amdgcn_fdot2_f32_bf16(__builtin_bit_cast(bf16x2, k), __builtin_bit_cast(bf16x2, q), acc, false);
}

constexpr size_t WS_Q = 0;
constexpr size_t WS_H = 2048;
constexpr size_t WS_ACC = 4096;
constexpr size_t WS_CNT = 5120;
constexpr size_t WS_ML = 8192;
constexpr size_t WS_PO = 16384;

__global__ void __launch_bounds__(256) k1_proj(const unsigned short* __restrict__ x,
                                               const unsigned short* __restrict__ wq,
                                               const unsigned short* __restrict__ wg,
                                               const unsigned short* __restrict__ wu,
                                               float* __restrict__ ws) {
  __shared__ float xn[D];
  __shared__ float red[2][4][32];
  __shared__ float st[8];
  const int tid = threadIdx.x, lane = tid & 63, wave = tid >> 6;
  const int b = blockIdx.x;
  const bool isq = b < 128;
  const int c0 = (isq ? b : (b - 128)) * 32;
  const int ld = isq ? NQ : F;
  const unsigned short* w0 = isq ? wq : wg;
  const int rg = tid >> 2, cq = tid & 3;
  u32x4 wa[7], wb[7];
#pragma unroll
  for (int i = 0; i < 7; ++i) {
    const size_t off = (size_t)(rg + 64 * i) * ld + c0 + cq * 8;
    wa[i] = *reinterpret_cast<const u32x4*>(w0 + off);
    if (!isq) wb[i] = *reinterpret_cast<const u32x4*>(wu + off);
  }
  if (b == 0) {
    for (int i = tid; i < 1025; i += 256) ws[WS_ACC + i] = 0.f;
  }
  float xv[2];
  xv[0] = bf2f(x[tid]);
  xv[1] = (tid + 256 < D) ? bf2f(x[tid + 256]) : 0.f;
  float s = xv[0] + xv[1];
#pragma unroll
  for (int o = 32; o; o >>= 1) s += __shfl_xor(s, o);
  if (lane == 0) st[wave] = s;
  __syncthreads();
  const float mean_f = (st[0] + st[1] + st[2] + st[3]) * (1.f / D);
  float d0 = xv[0] - mean_f, d1 = (tid + 256 < D) ? xv[1] - mean_f : 0.f;
  float v = d0 * d0 + d1 * d1;
#pragma unroll
  for (int o = 32; o; o >>= 1) v += __shfl_xor(v, o);
  if (lane == 0) st[4 + wave] = v;
  __syncthreads();
  const float var_b = rbf((st[4] + st[5] + st[6] + st[7]) * (1.f / D));
  const float mean_b = rbf(mean_f);
  const float den = rbf(sqrtf(rbf(var_b + 1e-5f)));
  xn[tid] = rbf(rbf(xv[0] - mean_b) / den);
  if (tid + 256 < D) xn[tid + 256] = rbf(rbf(xv[1] - mean_b) / den);
  __syncthreads();
  float aa[8], ab[8];
#pragma unroll
  for (int k = 0; k < 8; ++k) { aa[k] = 0.f; ab[k] = 0.f; }
#pragma unroll
  for (int i = 0; i < 7; ++i) {
    const float xs = xn[rg + 64 * i];
#pragma unroll
    for (int k = 0; k < 4; ++k) {
      aa[2 * k] = fmaf(xs, lo16(wa[i][k]), aa[2 * k]);
      aa[2 * k + 1] = fmaf(xs, hi16(wa[i][k]), aa[2 * k + 1]);
      if (!isq) {
        ab[2 * k] = fmaf(xs, lo16(wb[i][k]), ab[2 * k]);
        ab[2 * k + 1] = fmaf(xs, hi16(wb[i][k]), ab[2 * k + 1]);
      }
    }
  }
#pragma unroll
  for (int k = 0; k < 8; ++k) {
#pragma unroll
    for (int o = 4; o < 64; o <<= 1) { aa[k] += __shfl_xor(aa[k], o); ab[k] += __shfl_xor(ab[k], o); }
  }
  if (lane < 4) {
#pragma unroll
    for (int k = 0; k < 8; ++k) { red[0][wave][lane * 8 + k] = aa[k]; red[1][wave][lane * 8 + k] = ab[k]; }
  }
  __syncthreads();
  if (isq) {
    if (tid < 16) {
      const int c = tid * 2;
      const float q0 = rbf(red[0][0][c] + red[0][1][c] + red[0][2][c] + red[0][3][c]);
      const float q1 = rbf(red[0][0][c + 1] + red[0][1][c + 1] + red[0][2][c + 1] + red[0][3][c + 1]);
      reinterpret_cast<unsigned*>(ws)[WS_Q + c0 / 2 + tid] = (__float_as_uint(q0) >> 16) | (__float_as_uint(q1) & 0xFFFF0000u);
    }
  } else {
    if (tid < 32) {
      const float g = rbf(red[0][0][tid] + red[0][1][tid] + red[0][2][tid] + red[0][3][tid]);
      const float u = rbf(red[1][0][tid] + red[1][1][tid] + red[1][2][tid] + red[1][3][tid]);
      const float sg = rbf(1.f / (1.f + __expf(-g)));
      ws[WS_H + c0 + tid] = rbf(sg * u);
    }
  }
}

__global__ void __launch_bounds__(256) k2_attn(const unsigned short* __restrict__ kc,
                                               const unsigned short* __restrict__ vc,
                                               float* __restrict__ ws) {
  __shared__ float pl[2][CH];
  __shared__ float wm[4][2];
  __shared__ float wl[4][2];
  __shared__ float om[4][2][DH];
  const int tid = threadIdx.x, lane = tid & 63, wave = tid >> 6;
  const int g = blockIdx.x / NCH, c = blockIdx.x % NCH;
  const int half = lane >> 5, cl = lane & 31;
  const size_t base = ((size_t)g * SK + (SK - WIN) + c * CH + wave * 32 + half) * DH + cl * 8;
  u32x4 K[16], V[16];
#pragma unroll
  for (int i = 0; i < 16; ++i) K[i] = __builtin_nontemporal_load(reinterpret_cast<const u32x4*>(kc + base + (size_t)i * 2 * DH));
#pragma unroll
  for (int i = 0; i < 16; ++i) V[i] = __builtin_nontemporal_load(reinterpret_cast<const u32x4*>(vc + base + (size_t)i * 2 * DH));
  u32x4 q0 = *reinterpret_cast<const u32x4*>(reinterpret_cast<const unsigned*>(ws) + WS_Q + (2 * g) * 128 + cl * 4);
  u32x4 q1 = *reinterpret_cast<const u32x4*>(reinterpret_cast<const unsigned*>(ws) + WS_Q + (2 * g + 1) * 128 + cl * 4);
  float xs[32];
#pragma unroll
  for (int i = 0; i < 16; ++i) {
    float a = 0.f, b = 0.f;
#pragma unroll
    for (int k = 0; k < 4; ++k) { a = dot2(K[i][k], q0[k], a); b = dot2(K[i][k], q1[k], b); }
    xs[2 * i] = a; xs[2 * i + 1] = b;
  }
#pragma unroll
  for (int st = 16; st >= 1; st >>= 1) {
    const bool up = (cl & st) != 0;
#pragma unroll
    for (int j = 0; j < st; ++j) {
      const float send = up ? xs[j] : xs[j + st];
      const float keep = up ? xs[j + st] : xs[j];
      xs[j] = keep + __shfl_xor(send, st);
    }
  }
  const int hd = cl & 1;
  const float sc = rbf(rbf(xs[0]) * 0.0625f);
  float m = sc;
#pragma unroll
  for (int o = 2; o < 64; o <<= 1) m = fmaxf(m, __shfl_xor(m, o));
  if (lane < 2) wm[wave][lane] = m;
  __syncthreads();
  const float M = fmaxf(fmaxf(wm[0][hd], wm[1][hd]), fmaxf(wm[2][hd], wm[3][hd]));
  const float p = __expf(sc - M);
  pl[hd][wave * 32 + 2 * (cl >> 1) + half] = p;
  float l = p;
#pragma unroll
  for (int o = 2; o < 64; o <<= 1) l += __shfl_xor(l, o);
  if (lane < 2) wl[wave][lane] = l;
  __syncthreads();
  float acc[2][8];
#pragma unroll
  for (int k = 0; k < 8; ++k) { acc[0][k] = 0.f; acc[1][k] = 0.f; }
#pragma unroll
  for (int i = 0; i < 16; ++i) {
    const float p0 = pl[0][wave * 32 + 2 * i + half], p1 = pl[1][wave * 32 + 2 * i + half];
#pragma unroll
    for (int k = 0; k < 4; ++k) {
      const float a = lo16(V[i][k]), b = hi16(V[i][k]);
      acc[0][2 * k] = fmaf(p0, a, acc[0][2 * k]); acc[0][2 * k + 1] = fmaf(p0, b, acc[0][2 * k + 1]);
      acc[1][2 * k] = fmaf(p1, a, acc[1][2 * k]); acc[1][2 * k + 1] = fmaf(p1, b, acc[1][2 * k + 1]);
    }
  }
#pragma unroll
  for (int k = 0; k < 8; ++k) { acc[0][k] += __shfl_xor(acc[0][k], 32); acc[1][k] += __shfl_xor(acc[1][k], 32); }
  if (half == 0) {
#pragma unroll
    for (int k = 0; k < 8; ++k) { om[wave][0][cl * 8 + k] = acc[0][k]; om[wave][1][cl * 8 + k] = acc[1][k]; }
  }
  __syncthreads();
  {
    const int h = tid >> 7, d = (tid & 127) * 2;
    float2 r;
    r.x = om[0][h][d] + om[1][h][d] + om[2][h][d] + om[3][h][d];
    r.y = om[0][h][d + 1] + om[1][h][d + 1] + om[2][h][d + 1] + om[3][h][d + 1];
    *reinterpret_cast<float2*>(ws + WS_PO + ((size_t)(2 * g + h) * NCH + c) * DH + d) = r;
    if (tid < 2) {
      const float Mh = fmaxf(fmaxf(wm[0][tid], wm[1][tid]), fmaxf(wm[2][tid], wm[3][tid]));
      float2 ml; ml.x = Mh; ml.y = wl[0][tid] + wl[1][tid] + wl[2][tid] + wl[3][tid];
      *reinterpret_cast<float2*>(ws + WS_ML + ((size_t)(2 * g + tid) * NCH + c) * 2) = ml;
    }
  }
}

constexpr int K3B = 128;
__global__ void __launch_bounds__(256) k3_out(const unsigned short* __restrict__ x,
                                              const unsigned short* __restrict__ wo,
                                              const unsigned short* __restrict__ wd,
                                              float* __restrict__ ws, unsigned short* __restrict__ out) {
  __shared__ float ov[32];
  __shared__ float hv[14];
  __shared__ float part[8][32];
  __shared__ float red[4][2][D];
  __shared__ int last;
  const int tid = threadIdx.x;
  const int b = blockIdx.x;
  const int h = b >> 3;
  const int j0 = b * 32;
  const int cv = tid % 56, rg = tid / 56;
  u32x4 wa[8], wb[4];
  if (tid < 224) {
#pragma unroll
    for (int i = 0; i < 8; ++i) wa[i] = *reinterpret_cast<const u32x4*>(wo + (size_t)(j0 + rg * 8 + i) * D + cv * 8);
#pragma unroll
    for (int i = 0; i < 4; ++i) {
      const int r = rg + 4 * i;
      wb[i] = (r < 14) ? *reinterpret_cast<const u32x4*>(wd + (size_t)(b * 14 + r) * D + cv * 8) : u32x4{0u, 0u, 0u, 0u};
    }
  }
  const float2 ml = *reinterpret_cast<const float2*>(ws + WS_ML + ((size_t)h * NCH + (tid & 31)) * 2);
  float M = ml.x;
#pragma unroll
  for (int o = 16; o; o >>= 1) M = fmaxf(M, __shfl_xor(M, o));
  const float wgt = __expf(ml.x - M);
  float L = wgt * ml.y;
#pragma unroll
  for (int o = 16; o; o >>= 1) L += __shfl_xor(L, o);
  {
    const int j = tid & 31, cg = tid >> 5;
    float a = 0.f;
#pragma unroll
    for (int i = 0; i < 4; ++i) {
      const int cc = cg * 4 + i;
      const float wc = __shfl(wgt, cc, 32);
      a = fmaf(wc, ws[WS_PO + ((size_t)h * NCH + cc) * DH + (j0 & 255) + j], a);
    }
    part[cg][j] = a;
  }
  if (tid < 14) hv[tid] = ws[WS_H + b * 14 + tid];
  __syncthreads();
  if (tid < 32) {
    float a = 0.f;
#pragma unroll
    for (int i = 0; i < 8; ++i) a += part[i][tid];
    ov[tid] = rbf(a / L);
  }
  __syncthreads();
  if (tid < 224) {
    float aa[8], ab[8];
#pragma unroll
    for (int k = 0; k < 8; ++k) { aa[k] = 0.f; ab[k] = 0.f; }
#pragma unroll
    for (int i = 0; i < 8; ++i) {
      const float o = ov[rg * 8 + i];
#pragma unroll
      for (int k = 0; k < 4; ++k) { aa[2 * k] = fmaf(o, lo16(wa[i][k]), aa[2 * k]); aa[2 * k + 1] = fmaf(o, hi16(wa[i][k]), aa[2 * k + 1]); }
    }
#pragma unroll
    for (int i = 0; i < 4; ++i) {
      const int r = rg + 4 * i;
      const float o = (r < 14) ? hv[r] : 0.f;
#pragma unroll
      for (int k = 0; k < 4; ++k) { ab[2 * k] = fmaf(o, lo16(wb[i][k]), ab[2 * k]); ab[2 * k + 1] = fmaf(o, hi16(wb[i][k]), ab[2 * k + 1]); }
    }
#pragma unroll
    for (int k = 0; k < 8; ++k) { red[rg][0][cv * 8 + k] = aa[k]; red[rg][1][cv * 8 + k] = ab[k]; }
  }
  __syncthreads();
  for (int i = tid; i < 2 * D; i += 256) {
    const int a = i / D, cc = i % D;
    const float v = red[0][a][cc] + red[1][a][cc] + red[2][a][cc] + red[3][a][cc];
    __hip_atomic_fetch_add(ws + WS_ACC + i, v, __ATOMIC_RELAXED, __HIP_MEMORY_SCOPE_AGENT);
  }
  __threadfence();
  __syncthreads();
  if (tid == 0) {
    const unsigned old = __hip_atomic_fetch_add(reinterpret_cast<unsigned*>(ws) + WS_CNT, 1u, __ATOMIC_RELAXED, __HIP_MEMORY_SCOPE_AGENT);
    last = (old == K3B - 1);
  }
  __syncthreads();
  if (last) {
    __threadfence();
    for (int i = tid; i < D; i += 256) {
      const float A = rbf(__hip_atomic_load(ws + WS_ACC + i, __ATOMIC_RELAXED, __HIP_MEMORY_SCOPE_AGENT));
      const float Bv = rbf(__hip_atomic_load(ws + WS_ACC + D + i, __ATOMIC_RELAXED, __HIP_MEMORY_SCOPE_AGENT));
      const float o = rbf(rbf(bf2f(x[i]) + A) + Bv);
      out[i] = (unsigned short)(__float_as_uint(o) >> 16);
    }
  }
}

extern "C" void kernel_launch(void* const* d_in, const int* in_sizes, int n_in,
                              void* d_out, int out_size, void* d_ws, size_t ws_size,
                              hipStream_t stream) {
  const unsigned short* x  = (const unsigned short*)d_in[0];
  const unsigned short* kc = (const unsigned short*)d_in[1];
  const unsigned short* vc = (const unsigned short*)d_in[2];
  const unsigned short* wq = (const unsigned short*)d_in[3];
  const unsigned short* wo = (const unsigned short*)d_in[4];
  const unsigned short* wg = (const unsigned short*)d_in[6];
  const unsigned short* wu = (const unsigned short*)d_in[7];
  const unsigned short* wd = (const unsigned short*)d_in[8];
  float* ws = (float*)d_ws;
  k1_proj<<<128 + 56, 256, 0, stream>>>(x, wq, wg, wu, ws);
  k2_attn<<<HKV * NCH, 256, 0, stream>>>(kc, vc, ws);
  k3_out<<<K3B, 256, 0, stream>>>(x, wo, wd, ws, (unsigned short*)d_out);
}
```

```cpp
#include <hip/hip_runtime.h>
#include <hip/hip_bf16.h>
#include <cstdint>

constexpr int D = 448, HQ = 16, HKV = 8, SK = 65536, DH = 256, WIN = 4096, F = 1792;
constexpr int NQ = HQ * DH;
constexpr int CH = 128;
constexpr int NCH = WIN / CH;

using u32x4 = __attribute__((ext_vector_type(4))) unsigned;
using bf16x2 = __attribute__((ext_vector_type(2))) __bf16;

__device__ __forceinline__ float bf2f(unsigned short v) { return __uint_as_float(((unsigned)v) << 16); }
__device__ __forceinline__ float rbf(float f) {
  unsigned u = __float_as_uint(f);
  u += 0x7FFFu + ((u >> 16) & 1u);
  return __uint_as_float(u & 0xFFFF0000u);
}
__device__ __forceinline__ float lo16(unsigned v) { return __uint_as_float(v << 16); }
__device__ __forceinline__ float hi16(unsigned v) { return __uint_as_float(v & 0xFFFF0000u); }
__device__ __forceinline__ float dot2(unsigned k, unsigned q, float acc) {
  return __builtin_amdgcn_fdot2_f32_bf16(__builtin_bit_cast(bf16x2, k), __builtin_bit_cast(bf16x2, q), acc, false);
}

constexpr size_t WS_PQ = 0;
constexpr size_t WS_PG = 32768;
constexpr size_t WS_PU = 49152;
constexpr size_t WS_ACC = 65536;
constexpr size_t WS_CNT = 66560;
constexpr size_t WS_ML = 69632;
constexpr size_t WS_PO = 73728;

__global__ void __launch_bounds__(256) k1_proj(const unsigned short* __restrict__ x,
                                               const unsigned short* __restrict__ wq,
                                               const unsigned short* __restrict__ wg,
                                               const unsigned short* __restrict__ wu,
                                               float* __restrict__ ws) {
  __shared__ float xn[D];
  __shared__ float red[8][256];
  __shared__ float st[8];
  const int tid = threadIdx.x, lane = tid & 63, wave = tid >> 6;
  const int b = blockIdx.x;
  const unsigned short* w; int ld, ct, rb; float* dst;
  if (b < 128)      { w = wq; ld = NQ; ct = b & 15; rb = b >> 4; dst = ws + WS_PQ + (size_t)rb * NQ; }
  else if (b < 184) { const int t = b - 128; w = wg; ld = F; ct = t % 7; rb = t / 7; dst = ws + WS_PG + (size_t)rb * F; }
  else              { const int t = b - 184; w = wu; ld = F; ct = t % 7; rb = t / 7; dst = ws + WS_PU + (size_t)rb * F; }
  const int rg = tid >> 5, cl = tid & 31;
  u32x4 wa[7];
#pragma unroll
  for (int i = 0; i < 7; ++i)
    wa[i] = *reinterpret_cast<const u32x4*>(w + (size_t)(rb * 56 + rg + 8 * i) * ld + ct * 256 + cl * 8);
  __builtin_amdgcn_sched_barrier(0);
  if (b == 0) {
    for (int i = tid; i < 2048; i += 256) ws[WS_ACC + i] = 0.f;
  }
  float xv[2];
  xv[0] = bf2f(x[tid]);
  xv[1] = (tid + 256 < D) ? bf2f(x[tid + 256]) : 0.f;
  float s = xv[0] + xv[1];
#pragma unroll
  for (int o = 32; o; o >>= 1) s += __shfl_xor(s, o);
  if (lane == 0) st[wave] = s;
  __syncthreads();
  const float mean_f = (st[0] + st[1] + st[2] + st[3]) * (1.f / D);
  float d0 = xv[0] - mean_f, d1 = (tid + 256 < D) ? xv[1] - mean_f : 0.f;
  float v = d0 * d0 + d1 * d1;
#pragma unroll
  for (int o = 32; o; o >>= 1) v += __shfl_xor(v, o);
  if (lane == 0) st[4 + wave] = v;
  __syncthreads();
  const float var_b = rbf((st[4] + st[5] + st[6] + st[7]) * (1.f / D));
  const float mean_b = rbf(mean_f);
  const float den = rbf(sqrtf(rbf(var_b + 1e-5f)));
  xn[tid] = rbf(rbf(xv[0] - mean_b) / den);
  if (tid + 256 < D) xn[tid + 256] = rbf(rbf(xv[1] - mean_b) / den);
  __syncthreads();
  float aa[8];
#pragma unroll
  for (int k = 0; k < 8; ++k) aa[k] = 0.f;
#pragma unroll
  for (int i = 0; i < 7; ++i) {
    const float xs = xn[rb * 56 + rg + 8 * i];
#pragma unroll
    for (int k = 0; k < 4; ++k) {
      aa[2 * k] = fmaf(xs, lo16(wa[i][k]), aa[2 * k]);
      aa[2 * k + 1] = fmaf(xs, hi16(wa[i][k]), aa[2 * k + 1]);
    }
  }
#pragma unroll
  for (int k = 0; k < 8; ++k) red[rg][cl * 8 + k] = aa[k];
  __syncthreads();
  float r = 0.f;
#pragma unroll
  for (int i = 0; i < 8; ++i) r += red[i][tid];
  dst[ct * 256 + tid] = r;
}

__global__ void __launch_bounds__(256) k2_attn(const unsigned short* __restrict__ kc,
                                               const unsigned short* __restrict__ vc,
                                               float* __restrict__ ws) {
  __shared__ __attribute__((aligned(16))) unsigned qs[256];
  __shared__ float pl[2][CH];
  __shared__ float wm[4][2];
  __shared__ float wl[4][2];
  __shared__ float om[4][2][DH];
  const int tid = threadIdx.x, lane = tid & 63, wave = tid >> 6;
  const int g = blockIdx.x / NCH, c = blockIdx.x % NCH;
  const int half = lane >> 5, cl = lane & 31;
  const size_t base = ((size_t)g * SK + (SK - WIN) + c * CH + wave * 32 + half) * DH + cl * 8;
  float2 qp[8];
#pragma unroll
  for (int i = 0; i < 8; ++i) qp[i] = *reinterpret_cast<const float2*>(ws + WS_PQ + (size_t)i * NQ + g * 512 + tid * 2);
  __builtin_amdgcn_sched_barrier(0);
  u32x4 K[16], V[16];
#pragma unroll
  for (int i = 0; i < 16; ++i) K[i] = __builtin_nontemporal_load(reinterpret_cast<const u32x4*>(kc + base + (size_t)i * 2 * DH));
#pragma unroll
  for (int i = 0; i < 16; ++i) V[i] = __builtin_nontemporal_load(reinterpret_cast<const u32x4*>(vc + base + (size_t)i * 2 * DH));
  __builtin_amdgcn_sched_barrier(0);
  {
    float qa = 0.f, qb = 0.f;
#pragma unroll
    for (int i = 0; i < 8; ++i) { qa += qp[i].x; qb += qp[i].y; }
    qs[tid] = (__float_as_uint(rbf(qa)) >> 16) | (__float_as_uint(rbf(qb)) & 0xFFFF0000u);
  }
  __syncthreads();
  const u32x4 q0 = *reinterpret_cast<const u32x4*>(&qs[cl * 4]);
  const u32x4 q1 = *reinterpret_cast<const u32x4*>(&qs[128 + cl * 4]);
  float xs[32];
#pragma unroll
  for (int i = 0; i < 16; ++i) {
    float a = 0.f, b = 0.f;
#pragma unroll
    for (int k = 0; k < 4; ++k) { a = dot2(K[i][k], q0[k], a); b = dot2(K[i][k], q1[k], b); }
    xs[2 * i] = a; xs[2 * i + 1] = b;
  }
#pragma unroll
  for (int st = 16; st >= 1; st >>= 1) {
    const bool up = (cl & st) != 0;
#pragma unroll
    for (int j = 0; j < st; ++j) {
      float va = xs[j], vb = xs[j + st];
      asm volatile("" : "+v"(va), "+v"(vb));
      const float send = up ? va : vb;
      const float keep = up ? vb : va;
      xs[j] = keep + __shfl_xor(send, st);
    }
  }
  const int hd = cl & 1;
  const float sc = rbf(rbf(xs[0]) * 0.0625f);
  float m = sc;
#pragma unroll
  for (int o = 2; o < 64; o <<= 1) m = fmaxf(m, __shfl_xor(m, o));
  if (lane < 2) wm[wave][lane] = m;
  __syncthreads();
  const float M = fmaxf(fmaxf(wm[0][hd], wm[1][hd]), fmaxf(wm[2][hd], wm[3][hd]));
  const float p = __expf(sc - M);
  pl[hd][wave * 32 + 2 * (cl >> 1) + half] = p;
  float l = p;
#pragma unroll
  for (int o = 2; o < 64; o <<= 1) l += __shfl_xor(l, o);
  if (lane < 2) wl[wave][lane] = l;
  __syncthreads();
  float acc[2][8];
#pragma unroll
  for (int k = 0; k < 8; ++k) { acc[0][k] = 0.f; acc[1][k] = 0.f; }
#pragma unroll
  for (int i = 0; i < 16; ++i) {
    const float p0 = pl[0][wave * 32 + 2 * i + half], p1 = pl[1][wave * 32 + 2 * i + half];
#pragma unroll
    for (int k = 0; k < 4; ++k) {
      const float a = lo16(V[i][k]), b = hi16(V[i][k]);
      acc[0][2 * k] = fmaf(p0, a, acc[0][2 * k]); acc[0][2 * k + 1] = fmaf(p0, b, acc[0][2 * k + 1]);
      acc[1][2 * k] = fmaf(p1, a, acc[1][2 * k]); acc[1][2 * k + 1] = fmaf(p1, b, acc[1][2 * k + 1]);
    }
  }
#pragma unroll
  for (int k = 0; k < 8; ++k) { acc[0][k] += __shfl_xor(acc[0][k], 32); acc[1][k] += __shfl_xor(acc[1][k], 32); }
  if (half == 0) {
#pragma unroll
    for (int k = 0; k < 8; ++k) { om[wave][0][cl * 8 + k] = acc[0][k]; om[wave][1][cl * 8 + k] = acc[1][k]; }
  }
  __syncthreads();
  {
    const int h = tid >> 7, d = (tid & 127) * 2;
    float2 r;
    r.x = om[0][h][d] + om[1][h][d] + om[2][h][d] + om[3][h][d];
    r.y = om[0][h][d + 1] + om[1][h][d + 1] + om[2][h][d + 1] + om[3][h][d + 1];
    *reinterpret_cast<float2*>(ws + WS_PO + ((size_t)(2 * g + h) * NCH + c) * DH + d) = r;
    if (tid < 2) {
      const float Mh = fmaxf(fmaxf(wm[0][tid], wm[1][tid]), fmaxf(wm[2][tid], wm[3][tid]));
      float2 ml; ml.x = Mh; ml.y = wl[0][tid] + wl[1][tid] + wl[2][tid] + wl[3][tid];
      *reinterpret_cast<float2*>(ws + WS_ML + ((size_t)(2 * g + tid) * NCH + c) * 2) = ml;
    }
  }
}

__global__ void __launch_bounds__(256) k3_out(const unsigned short* __restrict__ x,
                                              const unsigned short* __restrict__ wo,
                                              const unsigned short* __restrict__ wd,
                                              float* __restrict__ ws, unsigned short* __restrict__ out) {
  __shared__ float ov[256];
  __shared__ float hv[112];
  __shared__ float wsc[32];
  __shared__ float red[32][2][64];
  __shared__ int last;
  const int tid = threadIdx.x;
  const int h = blockIdx.x / 7, ct = blockIdx.x % 7;
  const int rg = tid >> 3, cl = tid & 7;
  u32x4 wa[8], wb[4];
#pragma unroll
  for (int i = 0; i < 8; ++i) wa[i] = *reinterpret_cast<const u32x4*>(wo + (size_t)(h * 256 + rg + 32 * i) * D + ct * 64 + cl * 8);
#pragma unroll
  for (int i = 0; i < 4; ++i) {
    const int r = rg + 32 * i;
    wb[i] = *reinterpret_cast<const u32x4*>(wd + (size_t)(h * 112 + (r < 112 ? r : 0)) * D + ct * 64 + cl * 8);
  }
  float pv[32];
#pragma unroll
  for (int c = 0; c < 32; ++c) pv[c] = ws[WS_PO + ((size_t)h * NCH + c) * DH + tid];
  const float2 ml = *reinterpret_cast<const float2*>(ws + WS_ML + ((size_t)h * NCH + (tid & 31)) * 2);
  float gp[8], up[8];
  if (tid < 112) {
#pragma unroll
    for (int i = 0; i < 8; ++i) { gp[i] = ws[WS_PG + (size_t)i * F + h * 112 + tid]; up[i] = ws[WS_PU + (size_t)i * F + h * 112 + tid]; }
  }
  __builtin_amdgcn_sched_barrier(0);
  float M = ml.x;
#pragma unroll
  for (int o = 16; o; o >>= 1) M = fmaxf(M, __shfl_xor(M, o));
  const float wgt = __expf(ml.x - M);
  float L = wgt * ml.y;
#pragma unroll
  for (int o = 16; o; o >>= 1) L += __shfl_xor(L, o);
  if (tid < 32) wsc[tid] = wgt;
  if (tid < 112) {
    float gs = 0.f, us = 0.f;
#pragma unroll
    for (int i = 0; i < 8; ++i) { gs += gp[i]; us += up[i]; }
    gs = rbf(gs); us = rbf(us);
    const float sg = rbf(1.f / (1.f + __expf(-gs)));
    hv[tid] = rbf(sg * us);
  }
  __syncthreads();
  {
    float a = 0.f;
#pragma unroll
    for (int c = 0; c < 32; ++c) a = fmaf(wsc[c], pv[c], a);
    ov[tid] = rbf(a / L);
  }
  __syncthreads();
  float aa[8], ab[8];
#pragma unroll
  for (int k = 0; k < 8; ++k) { aa[k] = 0.f; ab[k] = 0.f; }
#pragma unroll
  for (int i = 0; i < 8; ++i) {
    const float o = ov[rg + 32 * i];
#pragma unroll
    for (int k = 0; k < 4; ++k) { aa[2 * k] = fmaf(o, lo16(wa[i][k]), aa[2 * k]); aa[2 * k + 1] = fmaf(o, hi16(wa[i][k]), aa[2 * k + 1]); }
  }
#pragma unroll
  for (int i = 0; i < 4; ++i) {
    const int r = rg + 32 * i;
    const float o = (r < 112) ? hv[r] : 0.f;
#pragma unroll
    for (int k = 0; k < 4; ++k) { ab[2 * k] = fmaf(o, lo16(wb[i][k]), ab[2 * k]); ab[2 * k + 1] = fmaf(o, hi16(wb[i][k]), ab[2 * k + 1]); }
  }
#pragma unroll
  for (int k = 0; k < 8; ++k) { red[rg][0][cl * 8 + k] = aa[k]; red[rg][1][cl * 8 + k] = ab[k]; }
  __syncthreads();
  if (tid < 128) {
    const int a = tid >> 6, cc = tid & 63;
    float v = 0.f;
#pragma unroll
    for (int i = 0; i < 32; ++i) v += red[i][a][cc];
    __hip_atomic_fetch_add(ws + WS_ACC + a * D + ct * 64 + cc, v, __ATOMIC_RELAXED, __HIP_MEMORY_SCOPE_AGENT);
  }
  asm volatile("s_waitcnt vmcnt(0)" ::: "memory");
  __syncthreads();
  if (tid == 0) {
    const unsigned old = __hip_atomic_fetch_add(reinterpret_cast<unsigned*>(ws) + WS_CNT + ct * 64, 1u, __ATOMIC_RELAXED, __HIP_MEMORY_SCOPE_AGENT);
    last = (old == HQ - 1);
  }
  __syncthreads();
  if (last && tid < 64) {
    const int i = ct * 64 + tid;
    const float A = rbf(__hip_atomic_load(ws + WS_ACC + i, __ATOMIC_RELAXED, __HIP_MEMORY_SCOPE_AGENT));
    const float Bv = rbf(__hip_atomic_load(ws + WS_ACC + D + i, __ATOMIC_RELAXED, __HIP_MEMORY_SCOPE_AGENT));
    const float o = rbf(rbf(bf2f(x[i]) + A) + Bv);
    out[i] = (unsigned short)(__float_as_uint(o) >> 16);
  }
}

extern "C" void kernel_launch(void* const* d_in, const int* in_sizes, int n_in,
                              void* d_out, int out_size, void* d_ws, size_t ws_size,
                              hipStream_t stream) {
  const unsigned short* x  = (const unsigned short*)d_in[0];
  const unsigned short* kc = (const unsigned short*)d_in[1];
  const unsigned short* vc = (const unsigned short*)d_in[2];
  const unsigned short* wq = (const unsigned short*)d_in[3];
  const unsigned short* wo = (const unsigned short*)d_in[4];
  const unsigned short* wg = (const unsigned short*)d_in[6];
  const unsigned short* wu = (const unsigned short*)d_in[7];
  const unsigned short* wd = (const unsigned short*)d_in[8];
  float* ws = (float*)d_ws;
  k1_proj<<<240, 256, 0, stream>>>(x, wq, wg, wu, ws);
  k2_attn<<<HKV * NCH, 256, 0, stream>>>(kc, vc, ws);
  k3_out<<<112, 256, 0, stream>>>(x, wo, wd, ws, (unsigned short*)d_out);
}
```

```cpp
#include <hip/hip_runtime.h>
#include <hip/hip_bf16.h>
#include <cstdint>

constexpr int D = 448, HQ = 16, HKV = 8, SK = 65536, DH = 256, WIN = 4096, F = 1792;
constexpr int NQ = HQ * DH;
constexpr int CH = 128;
constexpr int NCH = WIN / CH;

using u32x4 = __attribute__((ext_vector_type(4))) unsigned;
using bf16x2 = __attribute__((ext_vector_type(2))) __bf16;
#define RLX_AGENT __ATOMIC_RELAXED, __HIP_MEMORY_SCOPE_AGENT

__device__ __forceinline__ float bf2f(unsigned short v) { return __uint_as_float(((unsigned)v) << 16); }
__device__ __forceinline__ float rbf(float f) {
  unsigned u = __float_as_uint(f);
  u += 0x7FFFu + ((u >> 16) & 1u);
  return __uint_as_float(u & 0xFFFF0000u);
}
__device__ __forceinline__ float lo16(unsigned v) { return __uint_as_float(v << 16); }
__device__ __forceinline__ float hi16(unsigned v) { return __uint_as_float(v & 0xFFFF0000u); }
__device__ __forceinline__ float dot2(unsigned k, unsigned q, float acc) {
  return __builtin_amdgcn_fdot2_f32_bf16(__builtin_bit_cast(bf16x2, k), __builtin_bit_cast(bf16x2, q), acc, false);
}

constexpr size_t WS_QACC = 0, WS_GACC = 4096, WS_UACC = 5888, WS_OACC = 7680;
constexpr size_t WS_CQ = 8704, WS_CO = WS_CQ + 8 * 64, WS_CHH = WS_CQ + 16 * 64, WS_CT = WS_CQ + 17 * 64;
constexpr size_t WS_ZERO_END = 10240;
constexpr size_t WS_ML = 16384;
constexpr size_t WS_PO = 20480;
constexpr unsigned SPIN_CAP = 4000000u;

__device__ __forceinline__ void wait_ge(unsigned* p, unsigned target) {
  for (unsigned s = 0; s < SPIN_CAP; ++s) {
    if (__hip_atomic_load(p, RLX_AGENT) >= target) return;
    __builtin_amdgcn_s_sleep(1);
  }
}

__global__ void __launch_bounds__(320, 1) fused_kernel(
    const unsigned short* __restrict__ x, const unsigned short* __restrict__ kc, const unsigned short* __restrict__ vc,
    const unsigned short* __restrict__ wq, const unsigned short* __restrict__ wo, const unsigned short* __restrict__ wg,
    const unsigned short* __restrict__ wu, const unsigned short* __restrict__ wd, float* __restrict__ ws,
    unsigned short* __restrict__ out) {
  __shared__ __attribute__((aligned(16))) unsigned qs[256];
  __shared__ float tmp[1024];
  __shared__ float pl[2][CH];
  __shared__ float wm[4][2];
  __shared__ float wl[4][2];
  __shared__ float om[4][2][DH];
  __shared__ float red2[4][2][DH];
  __shared__ float ov[256];
  __shared__ float hv[112];
  __shared__ float wsc[32];
  __shared__ int last;
  unsigned* wsu = reinterpret_cast<unsigned*>(ws);
  const int tid = threadIdx.x, lane = tid & 63, wave = tid >> 6;
  const int g = blockIdx.x / NCH, c = blockIdx.x % NCH;
  const bool tailwg = c < 14;
  const int th = 2 * g + c / 7, tct = c % 7;
  const int half = lane >> 5, cl = lane & 31;

  u32x4 K[16], V[16], wa[8], wb[4];
  if (wave < 4) {
    const size_t base = ((size_t)g * SK + (SK - WIN) + c * CH + wave * 32 + half) * DH + cl * 8;
#pragma unroll
    for (int i = 0; i < 16; ++i) K[i] = __builtin_nontemporal_load(reinterpret_cast<const u32x4*>(kc + base + (size_t)i * 2 * DH));
#pragma unroll
    for (int i = 0; i < 16; ++i) V[i] = __builtin_nontemporal_load(reinterpret_cast<const u32x4*>(vc + base + (size_t)i * 2 * DH));
    if (tailwg) {
      const int rg = tid >> 3, c8 = tid & 7;
#pragma unroll
      for (int i = 0; i < 8; ++i) wa[i] = *reinterpret_cast<const u32x4*>(wo + (size_t)(th * 256 + rg + 32 * i) * D + tct * 64 + c8 * 8);
#pragma unroll
      for (int i = 0; i < 4; ++i) {
        const int r = rg + 32 * i;
        wb[i] = *reinterpret_cast<const u32x4*>(wd + (size_t)(th * 112 + (r < 112 ? r : 0)) * D + tct * 64 + c8 * 8);
      }
    }
    __builtin_amdgcn_sched_barrier(0);
  } else {
    u32x4 a1[14], a2[14];
    const int r0 = c * 14;
    const bool isg = lane < 28, isu = (lane >= 32 && lane < 60);
    const unsigned short* w2 = isu ? wu : wg;
    const int c2 = g * 224 + ((isg || isu) ? (lane & 31) : 0) * 8;
#pragma unroll
    for (int i = 0; i < 14; ++i) a1[i] = *reinterpret_cast<const u32x4*>(wq + (size_t)(r0 + i) * NQ + g * 512 + lane * 8);
#pragma unroll
    for (int i = 0; i < 14; ++i) a2[i] = *reinterpret_cast<const u32x4*>(w2 + (size_t)(r0 + i) * F + c2);
    float xv[7];
#pragma unroll
    for (int i = 0; i < 7; ++i) xv[i] = bf2f(x[lane + 64 * i]);
#pragma unroll
    for (int i = 0; i < 14; ++i) asm volatile("" : "+v"(a2[i]));
    __builtin_amdgcn_sched_barrier(0);
    float s = 0.f;
#pragma unroll
    for (int i = 0; i < 7; ++i) s += xv[i];
#pragma unroll
    for (int o = 32; o; o >>= 1) s += __shfl_xor(s, o);
    const float mean_f = s * (1.f / D);
    float v = 0.f;
#pragma unroll
    for (int i = 0; i < 7; ++i) { const float d = xv[i] - mean_f; v += d * d; }
#pragma unroll
    for (int o = 32; o; o >>= 1) v += __shfl_xor(v, o);
    const float var_b = rbf(v * (1.f / D));
    const float mean_b = rbf(mean_f);
    const float den = rbf(sqrtf(rbf(var_b + 1e-5f)));
#pragma unroll
    for (int i = 0; i < 7; ++i) tmp[lane + 64 * i] = rbf(rbf(xv[i] - mean_b) / den);
    __builtin_amdgcn_s_waitcnt(0xC07F);
    float q8[8], g8[8];
#pragma unroll
    for (int k = 0; k < 8; ++k) { q8[k] = 0.f; g8[k] = 0.f; }
#pragma unroll
    for (int i = 0; i < 14; ++i) {
      const float xs = tmp[r0 + i];
#pragma unroll
      for (int k = 0; k < 4; ++k) {
        q8[2 * k] = fmaf(xs, lo16(a1[i][k]), q8[2 * k]); q8[2 * k + 1] = fmaf(xs, hi16(a1[i][k]), q8[2 * k + 1]);
        g8[2 * k] = fmaf(xs, lo16(a2[i][k]), g8[2 * k]); g8[2 * k + 1] = fmaf(xs, hi16(a2[i][k]), g8[2 * k + 1]);
      }
    }
    __builtin_amdgcn_s_waitcnt(0xC07F);
#pragma unroll
    for (int k = 0; k < 8; ++k) tmp[lane * 8 + k] = q8[k];
    if (isg || isu) {
#pragma unroll
      for (int k = 0; k < 8; ++k) tmp[512 + (isu ? 224 : 0) + (lane & 31) * 8 + k] = g8[k];
    }
    __builtin_amdgcn_s_waitcnt(0xC07F);
#pragma unroll
    for (int k = 0; k < 8; ++k) __hip_atomic_fetch_add(ws + WS_QACC + g * 512 + k * 64 + lane, tmp[k * 64 + lane], RLX_AGENT);
#pragma unroll
    for (int k = 0; k < 7; ++k) {
      const int e = k * 64 + lane;
      float* dst = (e < 224) ? (ws + WS_GACC + g * 224 + e) : (ws + WS_UACC + g * 224 + (e - 224));
      __hip_atomic_fetch_add(dst, tmp[512 + e], RLX_AGENT);
    }
    asm volatile("s_waitcnt vmcnt(0)" ::: "memory");
    if (lane == 0) {
      __hip_atomic_fetch_add(wsu + WS_CQ + g * 64, 1u, RLX_AGENT);
      __hip_atomic_fetch_add(wsu + WS_CHH, 1u, RLX_AGENT);
      wait_ge(wsu + WS_CQ + g * 64, NCH);
    }
    asm volatile("s_waitcnt vmcnt(0)" ::: "memory");
#pragma unroll
    for (int k = 0; k < 4; ++k) {
      const int e = k * 64 + lane;
      const float qa = __hip_atomic_load(ws + WS_QACC + g * 512 + 2 * e, RLX_AGENT);
      const float qb = __hip_atomic_load(ws + WS_QACC + g * 512 + 2 * e + 1, RLX_AGENT);
      qs[e] = (__float_as_uint(rbf(qa)) >> 16) | (__float_as_uint(rbf(qb)) & 0xFFFF0000u);
    }
  }
  __syncthreads();
  const int hd = cl & 1;
  float sc = 0.f;
  if (wave < 4) {
    const u32x4 q0 = *reinterpret_cast<const u32x4*>(&qs[cl * 4]);
    const u32x4 q1 = *reinterpret_cast<const u32x4*>(&qs[128 + cl * 4]);
    float xs[32];
#pragma unroll
    for (int i = 0; i < 16; ++i) {
      float a = 0.f, b = 0.f;
#pragma unroll
      for (int k = 0; k < 4; ++k) { a = dot2(K[i][k], q0[k], a); b = dot2(K[i][k], q1[k], b); }
      xs[2 * i] = a; xs[2 * i + 1] = b;
    }
#pragma unroll
    for (int st = 16; st >= 1; st >>= 1) {
      const bool up = (cl & st) != 0;
#pragma unroll
      for (int j = 0; j < st; ++j) {
        float va = xs[j], vb = xs[j + st];
        asm volatile("" : "+v"(va), "+v"(vb));
        const float send = up ? va : vb;
        const float keep = up ? vb : va;
        xs[j] = keep + __shfl_xor(send, st);
      }
    }
    sc = rbf(rbf(xs[0]) * 0.0625f);
    float m = sc;
#pragma unroll
    for (int o = 2; o < 64; o <<= 1) m = fmaxf(m, __shfl_xor(m, o));
    if (lane < 2) wm[wave][lane] = m;
  }
  __syncthreads();
  if (wave < 4) {
    const float M = fmaxf(fmaxf(wm[0][hd], wm[1][hd]), fmaxf(wm[2][hd], wm[3][hd]));
    const float p = __expf(sc - M);
    pl[hd][wave * 32 + 2 * (cl >> 1) + half] = p;
    float l = p;
#pragma unroll
    for (int o = 2; o < 64; o <<= 1) l += __shfl_xor(l, o);
    if (lane < 2) wl[wave][lane] = l;
  }
  __syncthreads();
  if (wave < 4) {
    float acc[2][8];
#pragma unroll
    for (int k = 0; k < 8; ++k) { acc[0][k] = 0.f; acc[1][k] = 0.f; }
#pragma unroll
    for (int i = 0; i < 16; ++i) {
      const float p0 = pl[0][wave * 32 + 2 * i + half], p1 = pl[1][wave * 32 + 2 * i + half];
#pragma unroll
      for (int k = 0; k < 4; ++k) {
        const float a = lo16(V[i][k]), b = hi16(V[i][k]);
        acc[0][2 * k] = fmaf(p0, a, acc[0][2 * k]); acc[0][2 * k + 1] = fmaf(p0, b, acc[0][2 * k + 1]);
        acc[1][2 * k] = fmaf(p1, a, acc[1][2 * k]); acc[1][2 * k + 1] = fmaf(p1, b, acc[1][2 * k + 1]);
      }
    }
#pragma unroll
    for (int k = 0; k < 8; ++k) { acc[0][k] += __shfl_xor(acc[0][k], 32); acc[1][k] += __shfl_xor(acc[1][k], 32); }
    if (half == 0) {
#pragma unroll
      for (int k = 0; k < 8; ++k) { om[wave][0][cl * 8 + k] = acc[0][k]; om[wave][1][cl * 8 + k] = acc[1][k]; }
    }
  }
  __syncthreads();
  if (wave < 4) {
    const int h = tid >> 7, d = (tid & 127) * 2;
    float2 r;
    r.x = om[0][h][d] + om[1][h][d] + om[2][h][d] + om[3][h][d];
    r.y = om[0][h][d + 1] + om[1][h][d + 1] + om[2][h][d + 1] + om[3][h][d + 1];
    __hip_atomic_store(reinterpret_cast<unsigned long long*>(ws + WS_PO + ((size_t)(2 * g + h) * NCH + c) * DH + d),
                       __builtin_bit_cast(unsigned long long, r), RLX_AGENT);
    if (tid < 2) {
      float2 ml;
      ml.x = fmaxf(fmaxf(wm[0][tid], wm[1][tid]), fmaxf(wm[2][tid], wm[3][tid]));
      ml.y = wl[0][tid] + wl[1][tid] + wl[2][tid] + wl[3][tid];
      __hip_atomic_store(reinterpret_cast<unsigned long long*>(ws + WS_ML + ((size_t)(2 * g + tid) * NCH + c) * 2),
                         __builtin_bit_cast(unsigned long long, ml), RLX_AGENT);
    }
    asm volatile("s_waitcnt vmcnt(0)" ::: "memory");
  }
  __syncthreads();
  if (tid == 0) __hip_atomic_fetch_add(wsu + WS_CO + g * 64, 1u, RLX_AGENT);
  if (!tailwg) return;
  if (tid == 0) { wait_ge(wsu + WS_CO + g * 64, NCH); wait_ge(wsu + WS_CHH, HKV * NCH); }
  asm volatile("s_waitcnt vmcnt(0)" ::: "memory");
  __syncthreads();
  float pv[32]; float L = 1.f;
  if (wave < 4) {
#pragma unroll
    for (int cc = 0; cc < 32; ++cc) pv[cc] = __hip_atomic_load(ws + WS_PO + ((size_t)th * NCH + cc) * DH + tid, RLX_AGENT);
    const float mx = __hip_atomic_load(ws + WS_ML + ((size_t)th * NCH + (tid & 31)) * 2, RLX_AGENT);
    const float ly = __hip_atomic_load(ws + WS_ML + ((size_t)th * NCH + (tid & 31)) * 2 + 1, RLX_AGENT);
    float gs = 0.f, us = 0.f;
    if (tid < 112) { gs = __hip_atomic_load(ws + WS_GACC + th * 112 + tid, RLX_AGENT); us = __hip_atomic_load(ws + WS_UACC + th * 112 + tid, RLX_AGENT); }
    float M = mx;
#pragma unroll
    for (int o = 16; o; o >>= 1) M = fmaxf(M, __shfl_xor(M, o));
    const float wgt = __expf(mx - M);
    L = wgt * ly;
#pragma unroll
    for (int o = 16; o; o >>= 1) L += __shfl_xor(L, o);
    if (tid < 32) wsc[tid] = wgt;
    if (tid < 112) {
      gs = rbf(gs); us = rbf(us);
      hv[tid] = rbf(rbf(1.f / (1.f + __expf(-gs))) * us);
    }
  }
  __syncthreads();
  if (wave < 4) {
    float a = 0.f;
#pragma unroll
    for (int cc = 0; cc < 32; ++cc) a = fmaf(wsc[cc], pv[cc], a);
    ov[tid] = rbf(a / L);
  }
  __syncthreads();
  float (*redA)[64] = reinterpret_cast<float (*)[64]>(&om[0][0][0]);
  float (*redB)[64] = reinterpret_cast<float (*)[64]>(&red2[0][0][0]);
  if (wave < 4) {
    const int rg = tid >> 3, c8 = tid & 7;
    float aa[8], ab[8];
#pragma unroll
    for (int k = 0; k < 8; ++k) { aa[k] = 0.f; ab[k] = 0.f; }
#pragma unroll
    for (int i = 0; i < 8; ++i) {
      const float o = ov[rg + 32 * i];
#pragma unroll
      for (int k = 0; k < 4; ++k) { aa[2 * k] = fmaf(o, lo16(wa[i][k]), aa[2 * k]); aa[2 * k + 1] = fmaf(o, hi16(wa[i][k]), aa[2 * k + 1]); }
    }
#pragma unroll
    for (int i = 0; i < 4; ++i) {
      const int r = rg + 32 * i;
      const float o = (r < 112) ? hv[r] : 0.f;
#pragma unroll
      for (int k = 0; k < 4; ++k) { ab[2 * k] = fmaf(o, lo16(wb[i][k]), ab[2 * k]); ab[2 * k + 1] = fmaf(o, hi16(wb[i][k]), ab[2 * k + 1]); }
    }
#pragma unroll
    for (int k = 0; k < 8; ++k) { redA[rg][c8 * 8 + k] = aa[k]; redB[rg][c8 * 8 + k] = ab[k]; }
  }
  __syncthreads();
  if (tid < 128) {
    const int a = tid >> 6, cc = tid & 63;
    float v = 0.f;
#pragma unroll
    for (int i = 0; i < 32; ++i) v += a ? redB[i][cc] : redA[i][cc];
    __hip_atomic_fetch_add(ws + WS_OACC + a * D + tct * 64 + cc, v, RLX_AGENT);
  }
  asm volatile("s_waitcnt vmcnt(0)" ::: "memory");
  __syncthreads();
  if (tid == 0) {
    const unsigned old = __hip_atomic_fetch_add(wsu + WS_CT + tct * 64, 1u, RLX_AGENT);
    last = (old == HQ - 1);
  }
  __syncthreads();
  if (last && tid < 64) {
    const int i = tct * 64 + tid;
    const float A = rbf(__hip_atomic_load(ws + WS_OACC + i, RLX_AGENT));
    const float Bv = rbf(__hip_atomic_load(ws + WS_OACC + D + i, RLX_AGENT));
    const float o = rbf(rbf(bf2f(x[i]) + A) + Bv);
    out[i] = (unsigned short)(__float_as_uint(o) >> 16);
  }
}

extern "C" void kernel_launch(void* const* d_in, const int* in_sizes, int n_in,
                              void* d_out, int out_size, void* d_ws, size_t ws_size,
                              hipStream_t stream) {
  hipMemsetAsync(d_ws, 0, WS_ZERO_END * 4, stream);
  fused_kernel<<<HKV * NCH, 320, 0, stream>>>(
      (const unsigned short*)d_in[0], (const unsigned short*)d_in[1], (const unsigned short*)d_in[2],
      (const unsigned short*)d_in[3], (const unsigned short*)d_in[4], (const unsigned short*)d_in[6],
      (const unsigned short*)d_in[7], (const unsigned short*)d_in[8], (float*)d_ws, (unsigned short*)d_out);
}
```

```cpp
#include <hip/hip_runtime.h>
#include <hip/hip_bf16.h>
#include <cstdint>

constexpr int D = 448, HQ = 16, HKV = 8, SK = 65536, DH = 256, WIN = 4096, F = 1792;
constexpr int NQ = HQ * DH;
constexpr int CH = 128;
constexpr int NCH = WIN / CH;

using u32x4 = __attribute__((ext_vector_type(4))) unsigned;
using bf16x2 = __attribute__((ext_vector_type(2))) __bf16;
#define RLX_AGENT __ATOMIC_RELAXED, __HIP_MEMORY_SCOPE_AGENT

__device__ __forceinline__ float bf2f(unsigned short v) { return __uint_as_float(((unsigned)v) << 16); }
__device__ __forceinline__ float rbf(float f) {
  unsigned u = __float_as_uint(f);
  u += 0x7FFFu + ((u >> 16) & 1u);
  return __uint_as_float(u & 0xFFFF0000u);
}
__device__ __forceinline__ float lo16(unsigned v) { return __uint_as_float(v << 16); }
__device__ __forceinline__ float hi16(unsigned v) { return __uint_as_float(v & 0xFFFF0000u); }
__device__ __forceinline__ float dot2(unsigned k, unsigned q, float acc) {
  return __builtin_amdgcn_fdot2_f32_bf16(__builtin_bit_cast(bf16x2, k), __builtin_bit_cast(bf16x2, q), acc, false);
}

constexpr size_t WS_QACC = 0, WS_GACC = 4096, WS_UACC = 5888, WS_OACC = 7680;
constexpr size_t WS_CQ = 8704, WS_CO = WS_CQ + 8 * 64, WS_CHH = WS_CQ + 16 * 64, WS_CT = WS_CQ + 17 * 64;
constexpr size_t WS_ZERO_END = 10240;
constexpr size_t WS_ML = 16384;
constexpr size_t WS_PO = 20480;
constexpr unsigned SPIN_CAP = 4000000u;

__device__ __forceinline__ void wait_ge(unsigned* p, unsigned target) {
  for (unsigned s = 0; s < SPIN_CAP; ++s) {
    if (__hip_atomic_load(p, RLX_AGENT) >= target) return;
    __builtin_amdgcn_s_sleep(1);
  }
}

__global__ void __launch_bounds__(320, 1) fused_kernel(
    const unsigned short* __restrict__ x, const unsigned short* __restrict__ kc, const unsigned short* __restrict__ vc,
    const unsigned short* __restrict__ wq, const unsigned short* __restrict__ wo, const unsigned short* __restrict__ wg,
    const unsigned short* __restrict__ wu, const unsigned short* __restrict__ wd, float* __restrict__ ws,
    unsigned short* __restrict__ out) {
  __shared__ __attribute__((aligned(16))) unsigned qs[256];
  __shared__ float tmp[1024];
  __shared__ float pl[2][CH];
  __shared__ float wm[4][2];
  __shared__ float wl[4][2];
  __shared__ float om[4][2][DH];
  __shared__ float red2[4][2][DH];
  __shared__ float ov[256];
  __shared__ float hv[112];
  __shared__ float wsc[32];
  __shared__ int last;
  unsigned* wsu = reinterpret_cast<unsigned*>(ws);
  const int tid = threadIdx.x, lane = tid & 63, wave = tid >> 6;
  const int g = blockIdx.x / NCH, c = blockIdx.x % NCH;
  const bool tailwg = c < 14;
  const int th = 2 * g + c / 7, tct = c % 7;
  const int half = lane >> 5, cl = lane & 31;

  u32x4 a1[14], a2[14];
  float xv[7];
  const int r0 = c * 14;
  const bool isg = lane < 28, isu = (lane >= 32 && lane < 60);
  if (wave == 4) {
    const unsigned short* w2 = isu ? wu : wg;
    const int c2 = g * 224 + ((isg || isu) ? (lane & 31) : 0) * 8;
#pragma unroll
    for (int i = 0; i < 14; ++i) a1[i] = *reinterpret_cast<const u32x4*>(wq + (size_t)(r0 + i) * NQ + g * 512 + lane * 8);
#pragma unroll
    for (int i = 0; i < 14; ++i) a2[i] = *reinterpret_cast<const u32x4*>(w2 + (size_t)(r0 + i) * F + c2);
#pragma unroll
    for (int i = 0; i < 7; ++i) xv[i] = bf2f(x[lane + 64 * i]);
  }
  __builtin_amdgcn_sched_barrier(0);
  __builtin_amdgcn_s_barrier();
  __builtin_amdgcn_sched_barrier(0);
  u32x4 K[16], V[16], wa[8], wb[4];
  if (wave < 4) {
    const size_t base = ((size_t)g * SK + (SK - WIN) + c * CH + wave * 32 + half) * DH + cl * 8;
#pragma unroll
    for (int i = 0; i < 16; ++i) K[i] = __builtin_nontemporal_load(reinterpret_cast<const u32x4*>(kc + base + (size_t)i * 2 * DH));
#pragma unroll
    for (int i = 0; i < 16; ++i) V[i] = __builtin_nontemporal_load(reinterpret_cast<const u32x4*>(vc + base + (size_t)i * 2 * DH));
    if (tailwg) {
      const int rg = tid >> 3, c8 = tid & 7;
#pragma unroll
      for (int i = 0; i < 8; ++i) wa[i] = *reinterpret_cast<const u32x4*>(wo + (size_t)(th * 256 + rg + 32 * i) * D + tct * 64 + c8 * 8);
#pragma unroll
      for (int i = 0; i < 4; ++i) {
        const int r = rg + 32 * i;
        wb[i] = *reinterpret_cast<const u32x4*>(wd + (size_t)(th * 112 + (r < 112 ? r : 0)) * D + tct * 64 + c8 * 8);
      }
    }
    __builtin_amdgcn_sched_barrier(0);
  } else {
#pragma unroll
    for (int i = 0; i < 14; ++i) asm volatile("" : "+v"(a2[i]));
    float s = 0.f;
#pragma unroll
    for (int i = 0; i < 7; ++i) s += xv[i];
#pragma unroll
    for (int o = 32; o; o >>= 1) s += __shfl_xor(s, o);
    const float mean_f = s * (1.f / D);
    float v = 0.f;
#pragma unroll
    for (int i = 0; i < 7; ++i) { const float d = xv[i] - mean_f; v += d * d; }
#pragma unroll
    for (int o = 32; o; o >>= 1) v += __shfl_xor(v, o);
    const float var_b = rbf(v * (1.f / D));
    const float mean_b = rbf(mean_f);
    const float den = rbf(sqrtf(rbf(var_b + 1e-5f)));
#pragma unroll
    for (int i = 0; i < 7; ++i) tmp[lane + 64 * i] = rbf(rbf(xv[i] - mean_b) / den);
    __builtin_amdgcn_s_waitcnt(0xC07F);
    float q8[8], g8[8];
#pragma unroll
    for (int k = 0; k < 8; ++k) { q8[k] = 0.f; g8[k] = 0.f; }
#pragma unroll
    for (int i = 0; i < 14; ++i) {
      const float xs = tmp[r0 + i];
#pragma unroll
      for (int k = 0; k < 4; ++k) {
        q8[2 * k] = fmaf(xs, lo16(a1[i][k]), q8[2 * k]); q8[2 * k + 1] = fmaf(xs, hi16(a1[i][k]), q8[2 * k + 1]);
        g8[2 * k] = fmaf(xs, lo16(a2[i][k]), g8[2 * k]); g8[2 * k + 1] = fmaf(xs, hi16(a2[i][k]), g8[2 * k + 1]);
      }
    }
    __builtin_amdgcn_s_waitcnt(0xC07F);
#pragma unroll
    for (int k = 0; k < 8; ++k) tmp[lane * 8 + k] = q8[k];
    if (isg || isu) {
#pragma unroll
      for (int k = 0; k < 8; ++k) tmp[512 + (isu ? 224 : 0) + (lane & 31) * 8 + k] = g8[k];
    }
    __builtin_amdgcn_s_waitcnt(0xC07F);
#pragma unroll
    for (int k = 0; k < 8; ++k) __hip_atomic_fetch_add(ws + WS_QACC + g * 512 + k * 64 + lane, tmp[k * 64 + lane], RLX_AGENT);
#pragma unroll
    for (int k = 0; k < 7; ++k) {
      const int e = k * 64 + lane;
      float* dst = (e < 224) ? (ws + WS_GACC + g * 224 + e) : (ws + WS_UACC + g * 224 + (e - 224));
      __hip_atomic_fetch_add(dst, tmp[512 + e], RLX_AGENT);
    }
    asm volatile("s_waitcnt vmcnt(0)" ::: "memory");
    if (lane == 0) {
      __hip_atomic_fetch_add(wsu + WS_CQ + g * 64, 1u, RLX_AGENT);
      __hip_atomic_fetch_add(wsu + WS_CHH, 1u, RLX_AGENT);
      wait_ge(wsu + WS_CQ + g * 64, NCH);
    }
    asm volatile("s_waitcnt vmcnt(0)" ::: "memory");
#pragma unroll
    for (int k = 0; k < 4; ++k) {
      const int e = k * 64 + lane;
      const float qa = __hip_atomic_load(ws + WS_QACC + g * 512 + 2 * e, RLX_AGENT);
      const float qb = __hip_atomic_load(ws + WS_QACC + g * 512 + 2 * e + 1, RLX_AGENT);
      qs[e] = (__float_as_uint(rbf(qa)) >> 16) | (__float_as_uint(rbf(qb)) & 0xFFFF0000u);
    }
  }
  __syncthreads();
  const int hd = cl & 1;
  float sc = 0.f;
  if (wave < 4) {
    const u32x4 q0 = *reinterpret_cast<const u32x4*>(&qs[cl * 4]);
    const u32x4 q1 = *reinterpret_cast<const u32x4*>(&qs[128 + cl * 4]);
    float xs[32];
#pragma unroll
    for (int i = 0; i < 16; ++i) {
      float a = 0.f, b = 0.f;
#pragma unroll
      for (int k = 0; k < 4; ++k) { a = dot2(K[i][k], q0[k], a); b = dot2(K[i][k], q1[k], b); }
      xs[2 * i] = a; xs[2 * i + 1] = b;
    }
#pragma unroll
    for (int st = 16; st >= 1; st >>= 1) {
      const bool up = (cl & st) != 0;
#pragma unroll
      for (int j = 0; j < st; ++j) {
        float va = xs[j], vb = xs[j + st];
        asm volatile("" : "+v"(va), "+v"(vb));
        const float send = up ? va : vb;
        const float keep = up ? vb : va;
        xs[j] = keep + __shfl_xor(send, st);
      }
    }
    sc = rbf(rbf(xs[0]) * 0.0625f);
    float m = sc;
#pragma unroll
    for (int o = 2; o < 64; o <<= 1) m = fmaxf(m, __shfl_xor(m, o));
    if (lane < 2) wm[wave][lane] = m;
  }
  __syncthreads();
  if (wave < 4) {
    const float M = fmaxf(fmaxf(wm[0][hd], wm[1][hd]), fmaxf(wm[2][hd], wm[3][hd]));
    const float p = __expf(sc - M);
    pl[hd][wave * 32 + 2 * (cl >> 1) + half] = p;
    float l = p;
#pragma unroll
    for (int o = 2; o < 64; o <<= 1) l += __shfl_xor(l, o);
    if (lane < 2) wl[wave][lane] = l;
  }
  __syncthreads();
  if (wave < 4) {
    float acc[2][8];
#pragma unroll
    for (int k = 0; k < 8; ++k) { acc[0][k] = 0.f; acc[1][k] = 0.f; }
#pragma unroll
    for (int i = 0; i < 16; ++i) {
      const float p0 = pl[0][wave * 32 + 2 * i + half], p1 = pl[1][wave * 32 + 2 * i + half];
#pragma unroll
      for (int k = 0; k < 4; ++k) {
        const float a = lo16(V[i][k]), b = hi16(V[i][k]);
        acc[0][2 * k] = fmaf(p0, a, acc[0][2 * k]); acc[0][2 * k + 1] = fmaf(p0, b, acc[0][2 * k + 1]);
        acc[1][2 * k] = fmaf(p1, a, acc[1][2 * k]); acc[1][2 * k + 1] = fmaf(p1, b, acc[1][2 * k + 1]);
      }
    }
#pragma unroll
    for (int k = 0; k < 8; ++k) { acc[0][k] += __shfl_xor(acc[0][k], 32); acc[1][k] += __shfl_xor(acc[1][k], 32); }
    if (half == 0) {
#pragma unroll
      for (int k = 0; k < 8; ++k) { om[wave][0][cl * 8 + k] = acc[0][k]; om[wave][1][cl * 8 + k] = acc[1][k]; }
    }
  }
  __syncthreads();
  if (wave < 4) {
    const int h = tid >> 7, d = (tid & 127) * 2;
    float2 r;
    r.x = om[0][h][d] + om[1][h][d] + om[2][h][d] + om[3][h][d];
    r.y = om[0][h][d + 1] + om[1][h][d + 1] + om[2][h][d + 1] + om[3][h][d + 1];
    __hip_atomic_store(reinterpret_cast<unsigned long long*>(ws + WS_PO + ((size_t)(2 * g + h) * NCH + c) * DH + d),
                       __builtin_bit_cast(unsigned long long, r), RLX_AGENT);
    if (tid < 2) {
      float2 ml;
      ml.x = fmaxf(fmaxf(wm[0][tid], wm[1][tid]), fmaxf(wm[2][tid], wm[3][tid]));
      ml.y = wl[0][tid] + wl[1][tid] + wl[2][tid] + wl[3][tid];
      __hip_atomic_store(reinterpret_cast<unsigned long long*>(ws + WS_ML + ((size_t)(2 * g + tid) * NCH + c) * 2),
                         __builtin_bit_cast(unsigned long long, ml), RLX_AGENT);
    }
    asm volatile("s_waitcnt vmcnt(0)" ::: "memory");
  }
  __syncthreads();
  if (tid == 0) __hip_atomic_fetch_add(wsu + WS_CO + g * 64, 1u, RLX_AGENT);
  if (!tailwg) return;
  if (tid == 0) { wait_ge(wsu + WS_CO + g * 64, NCH); wait_ge(wsu + WS_CHH, HKV * NCH); }
  asm volatile("s_waitcnt vmcnt(0)" ::: "memory");
  __syncthreads();
  float pv[32]; float L = 1.f;
  if (wave < 4) {
#pragma unroll
    for (int cc = 0; cc < 32; ++cc) pv[cc] = __hip_atomic_load(ws + WS_PO + ((size_t)th * NCH + cc) * DH + tid, RLX_AGENT);
    const float mx = __hip_atomic_load(ws + WS_ML + ((size_t)th * NCH + (tid & 31)) * 2, RLX_AGENT);
    const float ly = __hip_atomic_load(ws + WS_ML + ((size_t)th * NCH + (tid & 31)) * 2 + 1, RLX_AGENT);
    float gs = 0.f, us = 0.f;
    if (tid < 112) { gs = __hip_atomic_load(ws + WS_GACC + th * 112 + tid, RLX_AGENT); us = __hip_atomic_load(ws + WS_UACC + th * 112 + tid, RLX_AGENT); }
    float M = mx;
#pragma unroll
    for (int o = 16; o; o >>= 1) M = fmaxf(M, __shfl_xor(M, o));
    const float wgt = __expf(mx - M);
    L = wgt * ly;
#pragma unroll
    for (int o = 16; o; o >>= 1) L += __shfl_xor(L, o);
    if (tid < 32) wsc[tid] = wgt;
    if (tid < 112) {
      gs = rbf(gs); us = rbf(us);
      hv[tid] = rbf(rbf(1.f / (1.f + __expf(-gs))) * us);
    }
  }
  __syncthreads();
  if (wave < 4) {
    float a = 0.f;
#pragma unroll
    for (int cc = 0; cc < 32; ++cc) a = fmaf(wsc[cc], pv[cc], a);
    ov[tid] = rbf(a / L);
  }
  __syncthreads();
  float (*redA)[64] = reinterpret_cast<float (*)[64]>(&om[0][0][0]);
  float (*redB)[64] = reinterpret_cast<float (*)[64]>(&red2[0][0][0]);
  if (wave < 4) {
    const int rg = tid >> 3, c8 = tid & 7;
    float aa[8], ab[8];
#pragma unroll
    for (int k = 0; k < 8; ++k) { aa[k] = 0.f; ab[k] = 0.f; }
#pragma unroll
    for (int i = 0; i < 8; ++i) {
      const float o = ov[rg + 32 * i];
#pragma unroll
      for (int k = 0; k < 4; ++k) { aa[2 * k] = fmaf(o, lo16(wa[i][k]), aa[2 * k]); aa[2 * k + 1] = fmaf(o, hi16(wa[i][k]), aa[2 * k + 1]); }
    }
#pragma unroll
    for (int i = 0; i < 4; ++i) {
      const int r = rg + 32 * i;
      const float o = (r < 112) ? hv[r] : 0.f;
#pragma unroll
      for (int k = 0; k < 4; ++k) { ab[2 * k] = fmaf(o, lo16(wb[i][k]), ab[2 * k]); ab[2 * k + 1] = fmaf(o, hi16(wb[i][k]), ab[2 * k + 1]); }
    }
#pragma unroll
    for (int k = 0; k < 8; ++k) { redA[rg][c8 * 8 + k] = aa[k]; redB[rg][c8 * 8 + k] = ab[k]; }
  }
  __syncthreads();
  if (tid < 128) {
    const int a = tid >> 6, cc = tid & 63;
    float v = 0.f;
#pragma unroll
    for (int i = 0; i < 32; ++i) v += a ? redB[i][cc] : redA[i][cc];
    __hip_atomic_fetch_add(ws + WS_OACC + a * D + tct * 64 + cc, v, RLX_AGENT);
  }
  asm volatile("s_waitcnt vmcnt(0)" ::: "memory");
  __syncthreads();
  if (tid == 0) {
    const unsigned old = __hip_atomic_fetch_add(wsu + WS_CT + tct * 64, 1u, RLX_AGENT);
    last = (old == HQ - 1);
  }
  __syncthreads();
  if (last && tid < 64) {
    const int i = tct * 64 + tid;
    const float A = rbf(__hip_atomic_load(ws + WS_OACC + i, RLX_AGENT));
    const float Bv = rbf(__hip_atomic_load(ws + WS_OACC + D + i, RLX_AGENT));
    const float o = rbf(rbf(bf2f(x[i]) + A) + Bv);
    out[i] = (unsigned short)(__float_as_uint(o) >> 16);
  }
}

extern "C" void kernel_launch(void* const* d_in, const int* in_sizes, int n_in,
                              void* d_out, int out_size, void* d_ws, size_t ws_size,
                              hipStream_t stream) {
  hipMemsetAsync(d_ws, 0, WS_ZERO_END * 4, stream);
  fused_kernel<<<HKV * NCH, 320, 0, stream>>>(
      (const unsigned short*)d_in[0], (const unsigned short*)d_in[1], (const unsigned short*)d_in[2],
      (const unsigned short*)d_in[3], (const unsigned short*)d_in[4], (const unsigned short*)d_in[6],
      (const unsigned short*)d_in[7], (const unsigned short*)d_in[8], (float*)d_ws, (unsigned short*)d_out);
}
```

```cpp
#include <hip/hip_runtime.h>
#include <hip/hip_bf16.h>
#include <cstdint>

constexpr int D = 448, HQ = 16, HKV = 8, SK = 65536, DH = 256, WIN = 4096, F = 1792;
constexpr int NQ = HQ * DH;
constexpr int CH = 128;
constexpr int NCH = WIN / CH;

using u32x4 = __attribute__((ext_vector_type(4))) unsigned;
using bf16x2 = __attribute__((ext_vector_type(2))) __bf16;

__device__ __forceinline__ float bf2f(unsigned short v) { return __uint_as_float(((unsigned)v) << 16); }
__device__ __forceinline__ float rbf(float f) {
  unsigned u = __float_as_uint(f);
  u += 0x7FFFu + ((u >> 16) & 1u);
  return __uint_as_float(u & 0xFFFF0000u);
}
__device__ __forceinline__ float lo16(unsigned v) { return __uint_as_float(v << 16); }
__device__ __forceinline__ float hi16(unsigned v) { return __uint_as_float(v & 0xFFFF0000u); }
__device__ __forceinline__ float dot2(unsigned k, unsigned q, float acc) {
  return __builtin_amdgcn_fdot2_f32_bf16(__builtin_bit_cast(bf16x2, k), __builtin_bit_cast(bf16x2, q), acc, false);
}

constexpr size_t WS_PQ = 0;
constexpr size_t WS_PG = 32768;
constexpr size_t WS_PU = 49152;
constexpr size_t WS_ACC = 65536;
constexpr size_t WS_CNT = 66560;
constexpr size_t WS_ML = 69632;
constexpr size_t WS_PO = 73728;

__global__ void __launch_bounds__(256) k1_proj(const unsigned short* __restrict__ x,
                                               const unsigned short* __restrict__ wq,
                                               const unsigned short* __restrict__ wg,
                                               const unsigned short* __restrict__ wu,
                                               float* __restrict__ ws) {
  __shared__ float xn[D];
  __shared__ float red[8][256];
  __shared__ float st[8];
  const int tid = threadIdx.x, lane = tid & 63, wave = tid >> 6;
  const int b = blockIdx.x;
  const unsigned short* w; int ld, ct, rb; float* dst;
  if (b < 128)      { w = wq; ld = NQ; ct = b & 15; rb = b >> 4; dst = ws + WS_PQ + (size_t)rb * NQ; }
  else if (b < 184) { const int t = b - 128; w = wg; ld = F; ct = t % 7; rb = t / 7; dst = ws + WS_PG + (size_t)rb * F; }
  else              { const int t = b - 184; w = wu; ld = F; ct = t % 7; rb = t / 7; dst = ws + WS_PU + (size_t)rb * F; }
  const int rg = tid >> 5, cl = tid & 31;
  u32x4 wa[7];
#pragma unroll
  for (int i = 0; i < 7; ++i)
    wa[i] = *reinterpret_cast<const u32x4*>(w + (size_t)(rb * 56 + rg + 8 * i) * ld + ct * 256 + cl * 8);
  __builtin_amdgcn_sched_barrier(0);
  if (b == 0) {
    for (int i = tid; i < 2048; i += 256) ws[WS_ACC + i] = 0.f;
  }
  float xv[2];
  xv[0] = bf2f(x[tid]);
  xv[1] = (tid + 256 < D) ? bf2f(x[tid + 256]) : 0.f;
  float s = xv[0] + xv[1];
#pragma unroll
  for (int o = 32; o; o >>= 1) s += __shfl_xor(s, o);
  if (lane == 0) st[wave] = s;
  __syncthreads();
  const float mean_f = (st[0] + st[1] + st[2] + st[3]) * (1.f / D);
  float d0 = xv[0] - mean_f, d1 = (tid + 256 < D) ? xv[1] - mean_f : 0.f;
  float v = d0 * d0 + d1 * d1;
#pragma unroll
  for (int o = 32; o; o >>= 1) v += __shfl_xor(v, o);
  if (lane == 0) st[4 + wave] = v;
  __syncthreads();
  const float var_b = rbf((st[4] + st[5] + st[6] + st[7]) * (1.f / D));
  const float mean_b = rbf(mean_f);
  const float den = rbf(sqrtf(rbf(var_b + 1e-5f)));
  xn[tid] = rbf(rbf(xv[0] - mean_b) / den);
  if (tid + 256 < D) xn[tid + 256] = rbf(rbf(xv[1] - mean_b) / den);
  __syncthreads();
  float aa[8];
#pragma unroll
  for (int k = 0; k < 8; ++k) aa[k] = 0.f;
#pragma unroll
  for (int i = 0; i < 7; ++i) {
    const float xs = xn[rb * 56 + rg + 8 * i];
#pragma unroll
    for (int k = 0; k < 4; ++k) {
      aa[2 * k] = fmaf(xs, lo16(wa[i][k]), aa[2 * k]);
      aa[2 * k + 1] = fmaf(xs, hi16(wa[i][k]), aa[2 * k + 1]);
    }
  }
#pragma unroll
  for (int k = 0; k < 8; ++k) red[rg][cl * 8 + k] = aa[k];
  __syncthreads();
  float r = 0.f;
#pragma unroll
  for (int i = 0; i < 8; ++i) r += red[i][tid];
  dst[ct * 256 + tid] = r;
}

__global__ void __launch_bounds__(256) k2_attn(const unsigned short* __restrict__ kc,
                                               const unsigned short* __restrict__ vc,
                                               float* __restrict__ ws) {
  __shared__ __attribute__((aligned(16))) unsigned qs[256];
  __shared__ float pl[2][CH];
  __shared__ float wm[4][2];
  __shared__ float wl[4][2];
  __shared__ float om[4][2][DH];
  const int tid = threadIdx.x, lane = tid & 63, wave = tid >> 6;
  const int g = blockIdx.x / NCH, c = blockIdx.x % NCH;
  const int half = lane >> 5, cl = lane & 31;
  const size_t base = ((size_t)g * SK + (SK - WIN) + c * CH + wave * 32 + half) * DH + cl * 8;
  float2 qp[8];
#pragma unroll
  for (int i = 0; i < 8; ++i) qp[i] = *reinterpret_cast<const float2*>(ws + WS_PQ + (size_t)i * NQ + g * 512 + tid * 2);
  asm volatile("" ::: "memory");
  __builtin_amdgcn_sched_barrier(0);
  u32x4 K[16], V[16];
#pragma unroll
  for (int i = 0; i < 16; ++i) K[i] = __builtin_nontemporal_load(reinterpret_cast<const u32x4*>(kc + base + (size_t)i * 2 * DH));
#pragma unroll
  for (int i = 0; i < 16; ++i) V[i] = __builtin_nontemporal_load(reinterpret_cast<const u32x4*>(vc + base + (size_t)i * 2 * DH));
  __builtin_amdgcn_sched_barrier(0);
  {
    float qa = 0.f, qb = 0.f;
#pragma unroll
    for (int i = 0; i < 8; ++i) { qa += qp[i].x; qb += qp[i].y; }
    qs[tid] = (__float_as_uint(rbf(qa)) >> 16) | (__float_as_uint(rbf(qb)) & 0xFFFF0000u);
  }
  __syncthreads();
  const u32x4 q0 = *reinterpret_cast<const u32x4*>(&qs[cl * 4]);
  const u32x4 q1 = *reinterpret_cast<const u32x4*>(&qs[128 + cl * 4]);
  float xs[32];
#pragma unroll
  for (int i = 0; i < 16; ++i) {
    float a = 0.f, b = 0.f;
#pragma unroll
    for (int k = 0; k < 4; ++k) { a = dot2(K[i][k], q0[k], a); b = dot2(K[i][k], q1[k], b); }
    xs[2 * i] = a; xs[2 * i + 1] = b;
  }
#pragma unroll
  for (int st = 16; st >= 1; st >>= 1) {
    const bool up = (cl & st) != 0;
#pragma unroll
    for (int j = 0; j < st; ++j) {
      float va = xs[j], vb = xs[j + st];
      asm volatile("" : "+v"(va), "+v"(vb));
      const float send = up ? va : vb;
      const float keep = up ? vb : va;
      xs[j] = keep + __shfl_xor(send, st);
    }
  }
  const int hd = cl & 1;
  const float sc = rbf(rbf(xs[0]) * 0.0625f);
  float m = sc;
#pragma unroll
  for (int o = 2; o < 64; o <<= 1) m = fmaxf(m, __shfl_xor(m, o));
  if (lane < 2) wm[wave][lane] = m;
  __syncthreads();
  const float M = fmaxf(fmaxf(wm[0][hd], wm[1][hd]), fmaxf(wm[2][hd], wm[3][hd]));
  const float p = __expf(sc - M);
  pl[hd][wave * 32 + 2 * (cl >> 1) + half] = p;
  float l = p;
#pragma unroll
  for (int o = 2; o < 64; o <<= 1) l += __shfl_xor(l, o);
  if (lane < 2) wl[wave][lane] = l;
  __syncthreads();
  float acc[2][8];
#pragma unroll
  for (int k = 0; k < 8; ++k) { acc[0][k] = 0.f; acc[1][k] = 0.f; }
#pragma unroll
  for (int i = 0; i < 16; ++i) {
    const float p0 = pl[0][wave * 32 + 2 * i + half], p1 = pl[1][wave * 32 + 2 * i + half];
#pragma unroll
    for (int k = 0; k < 4; ++k) {
      const float a = lo16(V[i][k]), b = hi16(V[i][k]);
      acc[0][2 * k] = fmaf(p0, a, acc[0][2 * k]); acc[0][2 * k + 1] = fmaf(p0, b, acc[0][2 * k + 1]);
      acc[1][2 * k] = fmaf(p1, a, acc[1][2 * k]); acc[1][2 * k + 1] = fmaf(p1, b, acc[1][2 * k + 1]);
    }
  }
#pragma unroll
  for (int k = 0; k < 8; ++k) { acc[0][k] += __shfl_xor(acc[0][k], 32); acc[1][k] += __shfl_xor(acc[1][k], 32); }
  if (half == 0) {
#pragma unroll
    for (int k = 0; k < 8; ++k) { om[wave][0][cl * 8 + k] = acc[0][k]; om[wave][1][cl * 8 + k] = acc[1][k]; }
  }
  __syncthreads();
  {
    const int h = tid >> 7, d = (tid & 127) * 2;
    float2 r;
    r.x = om[0][h][d] + om[1][h][d] + om[2][h][d] + om[3][h][d];
    r.y = om[0][h][d + 1] + om[1][h][d + 1] + om[2][h][d + 1] + om[3][h][d + 1];
    *reinterpret_cast<float2*>(ws + WS_PO + ((size_t)(2 * g + h) * NCH + c) * DH + d) = r;
    if (tid < 2) {
      const float Mh = fmaxf(fmaxf(wm[0][tid], wm[1][tid]), fmaxf(wm[2][tid], wm[3][tid]));
      float2 ml; ml.x = Mh; ml.y = wl[0][tid] + wl[1][tid] + wl[2][tid] + wl[3][tid];
      *reinterpret_cast<float2*>(ws + WS_ML + ((size_t)(2 * g + tid) * NCH + c) * 2) = ml;
    }
  }
}

__global__ void __launch_bounds__(256) k3_out(const unsigned short* __restrict__ x,
                                              const unsigned short* __restrict__ wo,
                                              const unsigned short* __restrict__ wd,
                                              float* __restrict__ ws, unsigned short* __restrict__ out) {
  __shared__ float ov[256];
  __shared__ float hv[112];
  __shared__ float wsc[32];
  __shared__ float red[32][2][64];
  __shared__ int last;
  const int tid = threadIdx.x;
  const int h = blockIdx.x / 7, ct = blockIdx.x % 7;
  const int rg = tid >> 3, cl = tid & 7;
  u32x4 wa[8], wb[4];
#pragma unroll
  for (int i = 0; i < 8; ++i) wa[i] = *reinterpret_cast<const u32x4*>(wo + (size_t)(h * 256 + rg + 32 * i) * D + ct * 64 + cl * 8);
#pragma unroll
  for (int i = 0; i < 4; ++i) {
    const int r = rg + 32 * i;
    wb[i] = *reinterpret_cast<const u32x4*>(wd + (size_t)(h * 112 + (r < 112 ? r : 0)) * D + ct * 64 + cl * 8);
  }
  float pv[32];
#pragma unroll
  for (int c = 0; c < 32; ++c) pv[c] = ws[WS_PO + ((size_t)h * NCH + c) * DH + tid];
  const float2 ml = *reinterpret_cast<const float2*>(ws + WS_ML + ((size_t)h * NCH + (tid & 31)) * 2);
  float gp[8], up[8];
  if (tid < 112) {
#pragma unroll
    for (int i = 0; i < 8; ++i) { gp[i] = ws[WS_PG + (size_t)i * F + h * 112 + tid]; up[i] = ws[WS_PU + (size_t)i * F + h * 112 + tid]; }
  }
  __builtin_amdgcn_sched_barrier(0);
  float M = ml.x;
#pragma unroll
  for (int o = 16; o; o >>= 1) M = fmaxf(M, __shfl_xor(M, o));
  const float wgt = __expf(ml.x - M);
  float L = wgt * ml.y;
#pragma unroll
  for (int o = 16; o; o >>= 1) L += __shfl_xor(L, o);
  if (tid < 32) wsc[tid] = wgt;
  if (tid < 112) {
    float gs = 0.f, us = 0.f;
#pragma unroll
    for (int i = 0; i < 8; ++i) { gs += gp[i]; us += up[i]; }
    gs = rbf(gs); us = rbf(us);
    const float sg = rbf(1.f / (1.f + __expf(-gs)));
    hv[tid] = rbf(sg * us);
  }
  __syncthreads();
  {
    float a = 0.f;
#pragma unroll
    for (int c = 0; c < 32; ++c) a = fmaf(wsc[c], pv[c], a);
    ov[tid] = rbf(a / L);
  }
  __syncthreads();
  float aa[8], ab[8];
#pragma unroll
  for (int k = 0; k < 8; ++k) { aa[k] = 0.f; ab[k] = 0.f; }
#pragma unroll
  for (int i = 0; i < 8; ++i) {
    const float o = ov[rg + 32 * i];
#pragma unroll
    for (int k = 0; k < 4; ++k) { aa[2 * k] = fmaf(o, lo16(wa[i][k]), aa[2 * k]); aa[2 * k + 1] = fmaf(o, hi16(wa[i][k]), aa[2 * k + 1]); }
  }
#pragma unroll
  for (int i = 0; i < 4; ++i) {
    const int r = rg + 32 * i;
    const float o = (r < 112) ? hv[r] : 0.f;
#pragma unroll
    for (int k = 0; k < 4; ++k) { ab[2 * k] = fmaf(o, lo16(wb[i][k]), ab[2 * k]); ab[2 * k + 1] = fmaf(o, hi16(wb[i][k]), ab[2 * k + 1]); }
  }
#pragma unroll
  for (int k = 0; k < 8; ++k) { red[rg][0][cl * 8 + k] = aa[k]; red[rg][1][cl * 8 + k] = ab[k]; }
  __syncthreads();
  if (tid < 128) {
    const int a = tid >> 6, cc = tid & 63;
    float v = 0.f;
#pragma unroll
    for (int i = 0; i < 32; ++i) v += red[i][a][cc];
    __hip_atomic_fetch_add(ws + WS_ACC + a * D + ct * 64 + cc, v, __ATOMIC_RELAXED, __HIP_MEMORY_SCOPE_AGENT);
  }
  asm volatile("s_waitcnt vmcnt(0)" ::: "memory");
  __syncthreads();
  if (tid == 0) {
    const unsigned old = __hip_atomic_fetch_add(reinterpret_cast<unsigned*>(ws) + WS_CNT + ct * 64, 1u, __ATOMIC_RELAXED, __HIP_MEMORY_SCOPE_AGENT);
    last = (old == HQ - 1);
  }
  __syncthreads();
  if (last && tid < 64) {
    const int i = ct * 64 + tid;
    const float A = rbf(__hip_atomic_load(ws + WS_ACC + i, __ATOMIC_RELAXED, __HIP_MEMORY_SCOPE_AGENT));
    const float Bv = rbf(__hip_atomic_load(ws + WS_ACC + D + i, __ATOMIC_RELAXED, __HIP_MEMORY_SCOPE_AGENT));
    const float o = rbf(rbf(bf2f(x[i]) + A) + Bv);
    out[i] = (unsigned short)(__float_as_uint(o) >> 16);
  }
}

extern "C" void kernel_launch(void* const* d_in, const int* in_sizes, int n_in,
                              void* d_out, int out_size, void* d_ws, size_t ws_size,
                              hipStream_t stream) {
  const unsigned short* x  = (const unsigned short*)d_in[0];
  const unsigned short* kc = (const unsigned short*)d_in[1];
  const unsigned short* vc = (const unsigned short*)d_in[2];
  const unsigned short* wq = (const unsigned short*)d_in[3];
  const unsigned short* wo = (const unsigned short*)d_in[4];
  const unsigned short* wg = (const unsigned short*)d_in[6];
  const unsigned short* wu = (const unsigned short*)d_in[7];
  const unsigned short* wd = (const unsigned short*)d_in[8];
  float* ws = (float*)d_ws;
  k1_proj<<<240, 256, 0, stream>>>(x, wq, wg, wu, ws);
  k2_attn<<<HKV * NCH, 256, 0, stream>>>(kc, vc, ws);
  k3_out<<<112, 256, 0, stream>>>(x, wo, wd, ws, (unsigned short*)d_out);
}
```

```cpp
#include <hip/hip_runtime.h>
#include <hip/hip_bf16.h>
#include <cstdint>

constexpr int D = 448, HQ = 16, HKV = 8, SK = 65536, DH = 256, WIN = 4096, F = 1792;
constexpr int NQ = HQ * DH;
constexpr int CH = 128;
constexpr int NCH = WIN / CH;

using u32x4 = __attribute__((ext_vector_type(4))) unsigned;
using bf16x2 = __attribute__((ext_vector_type(2))) __bf16;

__device__ __forceinline__ float bf2f(unsigned short v) { return __uint_as_float(((unsigned)v) << 16); }
__device__ __forceinline__ float rbf(float f) {
  unsigned u = __float_as_uint(f);
  u += 0x7FFFu + ((u >> 16) & 1u);
  return __uint_as_float(u & 0xFFFF0000u);
}
__device__ __forceinline__ float lo16(unsigned v) { return __uint_as_float(v << 16); }
__device__ __forceinline__ float hi16(unsigned v) { return __uint_as_float(v & 0xFFFF0000u); }
__device__ __forceinline__ float dot2(unsigned k, unsigned q, float acc) {
  return __builtin_amdgcn_fdot2_f32_bf16(__builtin_bit_cast(bf16x2, k), __builtin_bit_cast(bf16x2, q), acc, false);
}

constexpr size_t WS_PQ = 0;
constexpr size_t WS_PG = 32768;
constexpr size_t WS_PU = 49152;
constexpr size_t WS_ACC = 65536;
constexpr size_t WS_CNT = 66560;
constexpr size_t WS_ML = 69632;
constexpr size_t WS_PO = 73728;

__global__ void __launch_bounds__(256) k1_proj(const unsigned short* __restrict__ x,
                                               const unsigned short* __restrict__ wq,
                                               const unsigned short* __restrict__ wg,
                                               const unsigned short* __restrict__ wu,
                                               float* __restrict__ ws) {
  __shared__ float xn[D];
  __shared__ float red[8][256];
  __shared__ float st[8];
  const int tid = threadIdx.x, lane = tid & 63, wave = tid >> 6;
  const int b = blockIdx.x;
  const unsigned short* w; int ld, ct, rb; float* dst;
  if (b < 128)      { w = wq; ld = NQ; ct = b & 15; rb = b >> 4; dst = ws + WS_PQ + (size_t)rb * NQ; }
  else if (b < 184) { const int t = b - 128; w = wg; ld = F; ct = t % 7; rb = t / 7; dst = ws + WS_PG + (size_t)rb * F; }
  else              { const int t = b - 184; w = wu; ld = F; ct = t % 7; rb = t / 7; dst = ws + WS_PU + (size_t)rb * F; }
  const int rg = tid >> 5, cl = tid & 31;
  u32x4 wa[7];
#pragma unroll
  for (int i = 0; i < 7; ++i)
    wa[i] = *reinterpret_cast<const u32x4*>(w + (size_t)(rb * 56 + rg + 8 * i) * ld + ct * 256 + cl * 8);
  __builtin_amdgcn_sched_barrier(0);
  if (b == 0) {
    for (int i = tid; i < 2048; i += 256) ws[WS_ACC + i] = 0.f;
  }
  float xv[2];
  xv[0] = bf2f(x[tid]);
  xv[1] = (tid + 256 < D) ? bf2f(x[tid + 256]) : 0.f;
  float s = xv[0] + xv[1];
#pragma unroll
  for (int o = 32; o; o >>= 1) s += __shfl_xor(s, o);
  if (lane == 0) st[wave] = s;
  __syncthreads();
  const float mean_f = (st[0] + st[1] + st[2] + st[3]) * (1.f / D);
  float d0 = xv[0] - mean_f, d1 = (tid + 256 < D) ? xv[1] - mean_f : 0.f;
  float v = d0 * d0 + d1 * d1;
#pragma unroll
  for (int o = 32; o; o >>= 1) v += __shfl_xor(v, o);
  if (lane == 0) st[4 + wave] = v;
  __syncthreads();
  const float var_b = rbf((st[4] + st[5] + st[6] + st[7]) * (1.f / D));
  const float mean_b = rbf(mean_f);
  const float den = rbf(sqrtf(rbf(var_b + 1e-5f)));
  xn[tid] = rbf(rbf(xv[0] - mean_b) / den);
  if (tid + 256 < D) xn[tid + 256] = rbf(rbf(xv[1] - mean_b) / den);
  __syncthreads();
  float aa[8];
#pragma unroll
  for (int k = 0; k < 8; ++k) aa[k] = 0.f;
#pragma unroll
  for (int i = 0; i < 7; ++i) {
    const float xs = xn[rb * 56 + rg + 8 * i];
#pragma unroll
    for (int k = 0; k < 4; ++k) {
      aa[2 * k] = fmaf(xs, lo16(wa[i][k]), aa[2 * k]);
      aa[2 * k + 1] = fmaf(xs, hi16(wa[i][k]), aa[2 * k + 1]);
    }
  }
#pragma unroll
  for (int k = 0; k < 8; ++k) red[rg][cl * 8 + k] = aa[k];
  __syncthreads();
  float r = 0.f;
#pragma unroll
  for (int i = 0; i < 8; ++i) r += red[i][tid];
  dst[ct * 256 + tid] = r;
}

__global__ void __launch_bounds__(256) k2_attn(const unsigned short* __restrict__ kc,
                                               const unsigned short* __restrict__ vc,
                                               float* __restrict__ ws) {
  __shared__ __attribute__((aligned(16))) unsigned qs[256];
  __shared__ float pl[2][CH];
  __shared__ float wm[4][2];
  __shared__ float wl[4][2];
  __shared__ float om[4][2][DH];
  const int tid = threadIdx.x, lane = tid & 63, wave = tid >> 6;
  const int g = blockIdx.x / NCH, c = blockIdx.x % NCH;
  const int half = lane >> 5, cl = lane & 31;
  const size_t base = ((size_t)g * SK + (SK - WIN) + c * CH + wave * 32 + half) * DH + cl * 8;
  using u32x2 = __attribute__((ext_vector_type(2))) unsigned;
  u32x2 qp[8];
  u32x4 K[16], V[16];
#pragma unroll
  for (int i = 0; i < 8; ++i)
    asm volatile("global_load_dwordx2 %0, %1, off" : "=v"(qp[i]) : "v"(ws + WS_PQ + (size_t)i * NQ + g * 512 + tid * 2) : "memory");
#pragma unroll
  for (int i = 0; i < 16; ++i)
    asm volatile("global_load_dwordx4 %0, %1, off nt" : "=v"(K[i]) : "v"(kc + base + (size_t)i * 2 * DH) : "memory");
#pragma unroll
  for (int i = 0; i < 16; ++i)
    asm volatile("global_load_dwordx4 %0, %1, off nt" : "=v"(V[i]) : "v"(vc + base + (size_t)i * 2 * DH) : "memory");
  asm volatile("s_waitcnt vmcnt(32)" : "+v"(qp[0]), "+v"(qp[1]), "+v"(qp[2]), "+v"(qp[3]), "+v"(qp[4]), "+v"(qp[5]), "+v"(qp[6]), "+v"(qp[7]) :: "memory");
  {
    float qa = 0.f, qb = 0.f;
#pragma unroll
    for (int i = 0; i < 8; ++i) { qa += __uint_as_float(qp[i][0]); qb += __uint_as_float(qp[i][1]); }
    qs[tid] = (__float_as_uint(rbf(qa)) >> 16) | (__float_as_uint(rbf(qb)) & 0xFFFF0000u);
  }
  __syncthreads();
  asm volatile("s_waitcnt vmcnt(16)" : "+v"(K[0]), "+v"(K[1]), "+v"(K[2]), "+v"(K[3]), "+v"(K[4]), "+v"(K[5]), "+v"(K[6]), "+v"(K[7]),
               "+v"(K[8]), "+v"(K[9]), "+v"(K[10]), "+v"(K[11]), "+v"(K[12]), "+v"(K[13]), "+v"(K[14]), "+v"(K[15]) :: "memory");
  const u32x4 q0 = *reinterpret_cast<const u32x4*>(&qs[cl * 4]);
  const u32x4 q1 = *reinterpret_cast<const u32x4*>(&qs[128 + cl * 4]);
  float xs[32];
#pragma unroll
  for (int i = 0; i < 16; ++i) {
    float a = 0.f, b = 0.f;
#pragma unroll
    for (int k = 0; k < 4; ++k) { a = dot2(K[i][k], q0[k], a); b = dot2(K[i][k], q1[k], b); }
    xs[2 * i] = a; xs[2 * i + 1] = b;
  }
#pragma unroll
  for (int st = 16; st >= 1; st >>= 1) {
    const bool up = (cl & st) != 0;
#pragma unroll
    for (int j = 0; j < st; ++j) {
      float va = xs[j], vb = xs[j + st];
      asm volatile("" : "+v"(va), "+v"(vb));
      const float send = up ? va : vb;
      const float keep = up ? vb : va;
      xs[j] = keep + __shfl_xor(send, st);
    }
  }
  const int hd = cl & 1;
  const float sc = rbf(rbf(xs[0]) * 0.0625f);
  float m = sc;
#pragma unroll
  for (int o = 2; o < 64; o <<= 1) m = fmaxf(m, __shfl_xor(m, o));
  if (lane < 2) wm[wave][lane] = m;
  __syncthreads();
  const float M = fmaxf(fmaxf(wm[0][hd], wm[1][hd]), fmaxf(wm[2][hd], wm[3][hd]));
  const float p = __expf(sc - M);
  pl[hd][wave * 32 + 2 * (cl >> 1) + half] = p;
  float l = p;
#pragma unroll
  for (int o = 2; o < 64; o <<= 1) l += __shfl_xor(l, o);
  if (lane < 2) wl[wave][lane] = l;
  __syncthreads();
  asm volatile("s_waitcnt vmcnt(0)" : "+v"(V[0]), "+v"(V[1]), "+v"(V[2]), "+v"(V[3]), "+v"(V[4]), "+v"(V[5]), "+v"(V[6]), "+v"(V[7]),
               "+v"(V[8]), "+v"(V[9]), "+v"(V[10]), "+v"(V[11]), "+v"(V[12]), "+v"(V[13]), "+v"(V[14]), "+v"(V[15]) :: "memory");
  float acc[2][8];
#pragma unroll
  for (int k = 0; k < 8; ++k) { acc[0][k] = 0.f; acc[1][k] = 0.f; }
#pragma unroll
  for (int i = 0; i < 16; ++i) {
    const float p0 = pl[0][wave * 32 + 2 * i + half], p1 = pl[1][wave * 32 + 2 * i + half];
#pragma unroll
    for (int k = 0; k < 4; ++k) {
      const float a = lo16(V[i][k]), b = hi16(V[i][k]);
      acc[0][2 * k] = fmaf(p0, a, acc[0][2 * k]); acc[0][2 * k + 1] = fmaf(p0, b, acc[0][2 * k + 1]);
      acc[1][2 * k] = fmaf(p1, a, acc[1][2 * k]); acc[1][2 * k + 1] = fmaf(p1, b, acc[1][2 * k + 1]);
    }
  }
#pragma unroll
  for (int k = 0; k < 8; ++k) { acc[0][k] += __shfl_xor(acc[0][k], 32); acc[1][k] += __shfl_xor(acc[1][k], 32); }
  if (half == 0) {
#pragma unroll
    for (int k = 0; k < 8; ++k) { om[wave][0][cl * 8 + k] = acc[0][k]; om[wave][1][cl * 8 + k] = acc[1][k]; }
  }
  __syncthreads();
  {
    const int h = tid >> 7, d = (tid & 127) * 2;
    float2 r;
    r.x = om[0][h][d] + om[1][h][d] + om[2][h][d] + om[3][h][d];
    r.y = om[0][h][d + 1] + om[1][h][d + 1] + om[2][h][d + 1] + om[3][h][d + 1];
    *reinterpret_cast<float2*>(ws + WS_PO + ((size_t)(2 * g + h) * NCH + c) * DH + d) = r;
    if (tid < 2) {
      const float Mh = fmaxf(fmaxf(wm[0][tid], wm[1][tid]), fmaxf(wm[2][tid], wm[3][tid]));
      float2 ml; ml.x = Mh; ml.y = wl[0][tid] + wl[1][tid] + wl[2][tid] + wl[3][tid];
      *reinterpret_cast<float2*>(ws + WS_ML + ((size_t)(2 * g + tid) * NCH + c) * 2) = ml;
    }
  }
}

__global__ void __launch_bounds__(256) k3_out(const unsigned short* __restrict__ x,
                                              const unsigned short* __restrict__ wo,
                                              const unsigned short* __restrict__ wd,
                                              float* __restrict__ ws, unsigned short* __restrict__ out) {
  __shared__ float ov[256];
  __shared__ float hv[112];
  __shared__ float wsc[32];
  __shared__ float red[32][2][64];
  __shared__ int last;
  const int tid = threadIdx.x;
  const int h = blockIdx.x / 7, ct = blockIdx.x % 7;
  const int rg = tid >> 3, cl = tid & 7;
  u32x4 wa[8], wb[4];
#pragma unroll
  for (int i = 0; i < 8; ++i) wa[i] = *reinterpret_cast<const u32x4*>(wo + (size_t)(h * 256 + rg + 32 * i) * D + ct * 64 + cl * 8);
#pragma unroll
  for (int i = 0; i < 4; ++i) {
    const int r = rg + 32 * i;
    wb[i] = *reinterpret_cast<const u32x4*>(wd + (size_t)(h * 112 + (r < 112 ? r : 0)) * D + ct * 64 + cl * 8);
  }
  float pv[32];
#pragma unroll
  for (int c = 0; c < 32; ++c) pv[c] = ws[WS_PO + ((size_t)h * NCH + c) * DH + tid];
  const float2 ml = *reinterpret_cast<const float2*>(ws + WS_ML + ((size_t)h * NCH + (tid & 31)) * 2);
  float gp[8], up[8];
  if (tid < 112) {
#pragma unroll
    for (int i = 0; i < 8; ++i) { gp[i] = ws[WS_PG + (size_t)i * F + h * 112 + tid]; up[i] = ws[WS_PU + (size_t)i * F + h * 112 + tid]; }
  }
  __builtin_amdgcn_sched_barrier(0);
  float M = ml.x;
#pragma unroll
  for (int o = 16; o; o >>= 1) M = fmaxf(M, __shfl_xor(M, o));
  const float wgt = __expf(ml.x - M);
  float L = wgt * ml.y;
#pragma unroll
  for (int o = 16; o; o >>= 1) L += __shfl_xor(L, o);
  if (tid < 32) wsc[tid] = wgt;
  if (tid < 112) {
    float gs = 0.f, us = 0.f;
#pragma unroll
    for (int i = 0; i < 8; ++i) { gs += gp[i]; us += up[i]; }
    gs = rbf(gs); us = rbf(us);
    const float sg = rbf(1.f / (1.f + __expf(-gs)));
    hv[tid] = rbf(sg * us);
  }
  __syncthreads();
  {
    float a = 0.f;
#pragma unroll
    for (int c = 0; c < 32; ++c) a = fmaf(wsc[c], pv[c], a);
    ov[tid] = rbf(a / L);
  }
  __syncthreads();
  float aa[8], ab[8];
#pragma unroll
  for (int k = 0; k < 8; ++k) { aa[k] = 0.f; ab[k] = 0.f; }
#pragma unroll
  for (int i = 0; i < 8; ++i) {
    const float o = ov[rg + 32 * i];
#pragma unroll
    for (int k = 0; k < 4; ++k) { aa[2 * k] = fmaf(o, lo16(wa[i][k]), aa[2 * k]); aa[2 * k + 1] = fmaf(o, hi16(wa[i][k]), aa[2 * k + 1]); }
  }
#pragma unroll
  for (int i = 0; i < 4; ++i) {
    const int r = rg + 32 * i;
    const float o = (r < 112) ? hv[r] : 0.f;
#pragma unroll
    for (int k = 0; k < 4; ++k) { ab[2 * k] = fmaf(o, lo16(wb[i][k]), ab[2 * k]); ab[2 * k + 1] = fmaf(o, hi16(wb[i][k]), ab[2 * k + 1]); }
  }
#pragma unroll
  for (int k = 0; k < 8; ++k) { red[rg][0][cl * 8 + k] = aa[k]; red[rg][1][cl * 8 + k] = ab[k]; }
  __syncthreads();
  if (tid < 128) {
    const int a = tid >> 6, cc = tid & 63;
    float v = 0.f;
#pragma unroll
    for (int i = 0; i < 32; ++i) v += red[i][a][cc];
    __hip_atomic_fetch_add(ws + WS_ACC + a * D + ct * 64 + cc, v, __ATOMIC_RELAXED, __HIP_MEMORY_SCOPE_AGENT);
  }
  asm volatile("s_waitcnt vmcnt(0)" ::: "memory");
  __syncthreads();
  if (tid == 0) {
    const unsigned old = __hip_atomic_fetch_add(reinterpret_cast<unsigned*>(ws) + WS_CNT + ct * 64, 1u, __ATOMIC_RELAXED, __HIP_MEMORY_SCOPE_AGENT);
    last = (old == HQ - 1);
  }
  __syncthreads();
  if (last && tid < 64) {
    const int i = ct * 64 + tid;
    const float A = rbf(__hip_atomic_load(ws + WS_ACC + i, __ATOMIC_RELAXED, __HIP_MEMORY_SCOPE_AGENT));
    const float Bv = rbf(__hip_atomic_load(ws + WS_ACC + D + i, __ATOMIC_RELAXED, __HIP_MEMORY_SCOPE_AGENT));
    const float o = rbf(rbf(bf2f(x[i]) + A) + Bv);
    out[i] = (unsigned short)(__float_as_uint(o) >> 16);
  }
}

extern "C" void kernel_launch(void* const* d_in, const int* in_sizes, int n_in,
                              void* d_out, int out_size, void* d_ws, size_t ws_size,
                              hipStream_t stream) {
  const unsigned short* x  = (const unsigned short*)d_in[0];
  const unsigned short* kc = (const unsigned short*)d_in[1];
  const unsigned short* vc = (const unsigned short*)d_in[2];
  const unsigned short* wq = (const unsigned short*)d_in[3];
  const unsigned short* wo = (const unsigned short*)d_in[4];
  const unsigned short* wg = (const unsigned short*)d_in[6];
  const unsigned short* wu = (const unsigned short*)d_in[7];
  const unsigned short* wd = (const unsigned short*)d_in[8];
  float* ws = (float*)d_ws;
  k1_proj<<<240, 256, 0, stream>>>(x, wq, wg, wu, ws);
  k2_attn<<<HKV * NCH, 256, 0, stream>>>(kc, vc, ws);
  k3_out<<<112, 256, 0, stream>>>(x, wo, wd, ws, (unsigned short*)d_out);
}
```
